```python
import torch

def _softmax(x, axis=-1):
    return torch.softmax(x, dim=axis)

def _gelu(x):
    return torch.nn.functional.gelu(x, approximate='tanh')

def _silu(x):
    return torch.nn.functional.silu(x)

def _sigmoid(x):
    return torch.sigmoid(x)

def _layernorm(x, axis=-1, eps=1e-05):
    m = x.mean(dim=axis, keepdim=True)
    v = x.var(dim=axis, keepdim=True, unbiased=False)
    return (x - m) / torch.sqrt(v + eps)

def _scatter_add(tgt, idx, src, axis=0):
    return tgt.clone().index_add_(axis, idx, src)

def _segment_sum(src, seg, n):
    out = torch.zeros((n,) + src.shape[1:], dtype=src.dtype)
    return out.index_add_(0, seg, src)

def _pad(x, pw):
    flat = tuple((v for pair in reversed(pw) for v in pair))
    return torch.nn.functional.pad(x, flat)
kernel_name = 'adaLN_cond__linear_kernel_hedgehog__glu_gated_glu__prompt'

def setup_inputs():

    _w = lambda a: (lambda t: t.to(torch.bfloat16) if t.is_floating_point() else t)(torch.as_tensor(a))

    def _A_setup():
        B, S, D, H = (1, 2048, 512, 32)
        Dh = D // H
        return {'x': _w((lambda *s: torch.randn(*s, dtype=torch.bfloat16) * 0.05)(B, S, D)), 'wqkv': _w((lambda *s: torch.randn(*s, dtype=torch.bfloat16) * 0.4)(D, 3 * D)), 'w_phi': _w((lambda *s: torch.randn(*s, dtype=torch.bfloat16) * 0.4)(H, Dh, Dh) * Dh ** (-0.5)), 'wo': _w((lambda *s: torch.randn(*s, dtype=torch.bfloat16) * 0.05)(D, D))}
    _A_in = _A_setup()

    def _B_setup():
        B, S, D, F = (1, 2048, 512, 1376)
        return {'x': _w((lambda *s: torch.randn(*s, dtype=torch.bfloat16) * 0.05)(B, S, D)), 'w_gate': _w((lambda *s: torch.randn(*s, dtype=torch.bfloat16) * 0.1)(D, F)), 'w_up': _w((lambda *s: torch.randn(*s, dtype=torch.bfloat16) * 0.05)(D, F)), 'w_down': _w((lambda *s: torch.randn(*s, dtype=torch.bfloat16) * 0.05)(F, D))}
    _B_in = _B_setup()
    return {'A_x': _A_in['x'], 'A_wqkv': _A_in['wqkv'], 'A_w_phi': _A_in['w_phi'], 'A_wo': _A_in['wo'], 'B_x': _B_in['x'], 'B_w_gate': _B_in['w_gate'], 'B_w_up': _B_in['w_up'], 'B_w_down': _B_in['w_down'], 'c': _w((lambda *s: torch.randn(*s, dtype=torch.bfloat16) * 0.2)(1, 512) * 8.0), 'w_mod': _w((lambda *s: torch.randn(*s, dtype=torch.bfloat16) * 0.2)(512, 6 * 512) * 512 ** (-0.5) * 8.0)}

def reference(A_x, A_wqkv, A_w_phi, A_wo, B_x, B_w_gate, B_w_up, B_w_down, c, w_mod):

    def _A_ref(x, wqkv, w_phi, wo):
        B, S, D = x.shape
        H = 32
        Dh = D // H
        qkv = (lambda x, *d: x.permute(*d))((x @ wqkv).reshape(B, S, 3, H, Dh), 2, 0, 3, 1, 4)
        q, k, v = (qkv[0], qkv[1], qkv[2])
        pq = torch.einsum('bhsd,hdf->bhsf', q, w_phi)
        pk = torch.einsum('bhsd,hdf->bhsf', k, w_phi)
        phi_q = torch.concatenate([_softmax(pq, axis=-1), _softmax(-pq, axis=-1)], axis=-1)
        phi_k = torch.concatenate([_softmax(pk, axis=-1), _softmax(-pk, axis=-1)], axis=-1)
        kv = torch.einsum('bhsf,bhsd->bhfd', phi_k, v)
        z = torch.einsum('bhsf->bhf', phi_k)
        num = torch.einsum('bhqf,bhfd->bhqd', phi_q, kv)
        den = torch.einsum('bhqf,bhf->bhq', phi_q, z)[..., None] + 1e-06
        o = num / den
        return (lambda x, *d: x.permute(*d))(o, 0, 2, 1, 3).reshape(B, S, D) @ wo

    def _B_ref(x, w_gate, w_up, w_down):
        g = torch.einsum('bsd,df->bsf', x, w_gate)
        u = torch.einsum('bsd,df->bsf', x, w_up)
        h = _sigmoid(g) * u
        return torch.einsum('bsf,fd->bsd', h, w_down)
    x = A_x
    mod = (_silu(c) @ w_mod).reshape(c.shape[0], 6, 1, -1)
    sa, ca, ga, sb, cb, gb = (mod[:, 0].clone(), mod[:, 1].clone(), mod[:, 2].clone(), mod[:, 3].clone(), mod[:, 4].clone(), mod[:, 5].clone())
    h = _layernorm(x, axis=-1) * (1 + ca) + sa
    x = x + ga * _A_ref(h, A_wqkv, A_w_phi, A_wo)
    h = _layernorm(x, axis=-1) * (1 + cb) + sb
    x = x + gb * _B_ref(h, B_w_gate, B_w_up, B_w_down)
    return x
```

```cpp
#include <hip/hip_runtime.h>
#include <hip/hip_bf16.h>

typedef __attribute__((ext_vector_type(8))) __bf16 bf16x8;
typedef __attribute__((ext_vector_type(2))) __bf16 bf16x2;
typedef __attribute__((ext_vector_type(4))) short s16x4;
typedef __attribute__((ext_vector_type(4))) float f32x4;
typedef __attribute__((ext_vector_type(2))) float f32x2;
typedef __attribute__((ext_vector_type(4))) unsigned u32x4;
typedef __attribute__((ext_vector_type(2))) unsigned u32x2;
typedef __attribute__((address_space(1))) unsigned gu32;
typedef __attribute__((address_space(1))) unsigned long long gu64;
#define LAS __attribute__((address_space(3)))
#define RLX_AGENT __ATOMIC_RELAXED, __HIP_MEMORY_SCOPE_AGENT
#define DEV __device__ __forceinline__

constexpr unsigned WS_FLAG = 4096;
constexpr unsigned WS_MOD = WS_FLAG + 4 * 256 * 128;
constexpr unsigned WS_KV = WS_MOD + 16384;
constexpr unsigned WS_ST = WS_KV + 256 * 4352;
constexpr unsigned WS_O = WS_ST + 2048 * 128;
constexpr unsigned WS_X1 = WS_O + 2097152;
constexpr unsigned WS_H = WS_X1 + 2097152;
constexpr unsigned WS_END = WS_H + 128 * 43 * 1024;
constexpr unsigned SLOT = 12288, L_MOD = 147456, L_SCR = 159744;
constexpr unsigned SPIN_CAP = 1u << 19;

DEV unsigned pk2(float a, float b) { f32x2 v = {a, b}; return __builtin_bit_cast(unsigned, __builtin_convertvector(v, bf16x2)); }
DEV float lo16(unsigned u) { return __uint_as_float(u << 16); }
DEV float hi16(unsigned u) { return __uint_as_float(u & 0xffff0000u); }
DEV float rbf(float a) { return lo16(pk2(a, a)); }
DEV s16x4 pk4(f32x4 v) { u32x2 r = {pk2(v[0], v[1]), pk2(v[2], v[3])}; return __builtin_bit_cast(s16x4, r); }
DEV f32x4 mfma32(u32x4 a, u32x4 b, f32x4 c) {
  return __builtin_amdgcn_mfma_f32_16x16x32_bf16(__builtin_bit_cast(bf16x8, a), __builtin_bit_cast(bf16x8, b), c, 0, 0, 0); }
DEV f32x4 mfma16(s16x4 a, s16x4 b, f32x4 c) { return __builtin_amdgcn_mfma_f32_16x16x16bf16_1k(a, b, c, 0, 0, 0); }
#define XRED(name, OP) DEV float name(float x) { \
  auto s = __builtin_amdgcn_permlane16_swap(__float_as_uint(x), __float_as_uint(x), false, false); \
  x = OP(__uint_as_float(s[0]), __uint_as_float(s[1])); \
  auto t = __builtin_amdgcn_permlane32_swap(__float_as_uint(x), __float_as_uint(x), false, false); \
  return OP(__uint_as_float(t[0]), __uint_as_float(t[1])); }
DEV float addf(float a, float b) { return a + b; }
XRED(xsum, addf) XRED(xmax, fmaxf) XRED(xmin, fminf)
template <int N> DEV float ror(float x) { return __int_as_float(__builtin_amdgcn_update_dpp(0, __float_as_int(x), 0x120 + N, 0xf, 0xf, false)); }
#define RRED(name, OP) DEV float name(float x) { x = OP(x, ror<8>(x)); x = OP(x, ror<4>(x)); x = OP(x, ror<2>(x)); return OP(x, ror<1>(x)); }
RRED(rsum, addf) RRED(rmax, fmaxf) RRED(rmin, fminf)

DEV void glds(const void* g, unsigned lds) {
  unsigned keep;
  asm volatile("s_mov_b32 %0, m0\n\ts_mov_b32 m0, %2\n\ts_nop 0\n\tglobal_load_lds_dwordx4 %1, off\n\ts_mov_b32 m0, %0"
               : "=&s"(keep) : "v"(g), "s"(lds) : "memory");
}
DEV void wait_vm0() { asm volatile("s_waitcnt vmcnt(0)" ::: "memory"); }
DEV void bar() { asm volatile("s_waitcnt lgkmcnt(0)\n\ts_barrier" ::: "memory"); }
DEV u32x2 trd(unsigned a) { return __builtin_bit_cast(u32x2, __builtin_amdgcn_ds_read_tr16_b64_v4i16((LAS s16x4*)a)); }
DEV u32x4 frag(unsigned a, unsigned d) { u32x2 x = trd(a), y = trd(a + d); return u32x4{x[0], x[1], y[0], y[1]}; }
DEV f32x4 ldsf4(unsigned a) { return *(LAS f32x4*)a; }
DEV void stsf4(unsigned a, f32x4 v) { *(LAS f32x4*)a = v; }

DEV void wait_flags(char* ws, unsigned first, unsigned stride, unsigned tag, int wave, int lane) {
  if (wave == 0) {
    gu32* p = (gu32*)(ws + WS_FLAG) + (first + (lane & 15) * stride) * 32;
    for (unsigned s = 0; s < SPIN_CAP; ++s) {
      if (__all(__hip_atomic_load(p, RLX_AGENT) == tag)) break;
      __builtin_amdgcn_s_sleep(1);
    }
  }
  bar();
}
DEV void publish(char* ws, unsigned idx, unsigned tag) {
  wait_vm0(); bar();
  if (threadIdx.x == 0) __hip_atomic_store((gu32*)(ws + WS_FLAG) + idx * 32, tag, RLX_AGENT);
}
DEV unsigned poll_granule(char* ws, unsigned idx, unsigned tag) {
  gu64* p = (gu64*)(ws + WS_MOD) + idx; unsigned long long v = 0;
  for (unsigned s = 0; s < SPIN_CAP; ++s) {
    v = __hip_atomic_load(p, RLX_AGENT);
    if (__all((unsigned)(v >> 32) == tag)) break;
    __builtin_amdgcn_s_sleep(1);
  }
  return (unsigned)v;
}
#define ST(i)

__global__ void __launch_bounds__(512, 2)
adaln_block(const unsigned short* __restrict__ x, const unsigned short* __restrict__ wqkv, const unsigned short* __restrict__ wphi,
            const unsigned short* __restrict__ wo, const unsigned short* __restrict__ wg, const unsigned short* __restrict__ wu,
            const unsigned short* __restrict__ wd, const unsigned short* __restrict__ cin, const unsigned short* __restrict__ wmod,
            unsigned short* __restrict__ out, char* ws) {
  __shared__ __attribute__((aligned(16))) char smem[163840];
  const unsigned L0 = (unsigned)(unsigned long long)(LAS char*)smem;
  const int tid = threadIdx.x, lane = tid & 63, wave = __builtin_amdgcn_readfirstlane(tid >> 6);
  const int g = lane >> 4, r16 = lane & 15, q = r16 >> 2, p = r16 & 3;
  const int b = blockIdx.x, xc = b & 7, j = b >> 3;
  const int mt = (xc >> 1) * 4 + (j & 3), nt = (xc & 1) * 8 + (j >> 2);
  const int Mt = (xc >> 1) * 8 + (j & 7), Nt = (xc & 1) * 4 + (j >> 3);
  const int wm = wave & 3, wk = wave >> 2;
  const __amdgpu_buffer_rsrc_t rs = __builtin_amdgcn_make_buffer_rsrc(ws, 0, WS_END, 0x00020000);
  const unsigned wbase = L0 + wave * 1024;
  const float LOG2E = 1.4426950408889634f;
  ST(0);
  const unsigned tag = __builtin_amdgcn_readfirstlane(__hip_atomic_load((gu32*)ws, RLX_AGENT)) + 1u;

  u32x4 wv[4]; unsigned short cv[4];
  if (b < 96) {
#pragma unroll
    for (int i = 0; i < 4; ++i) { int row = (tid >> 2) + 128 * i; wv[i] = *(const u32x4*)(wmod + row * 3072 + b * 32 + (tid & 3) * 8); cv[i] = cin[row]; }
  }
  u32x4 xa[16];
  { const unsigned short* xp = x + (mt * 128 + wave * 16 + r16) * 512 + g * 8;
#pragma unroll
    for (int ks = 0; ks < 16; ++ks) xa[ks] = *(const u32x4*)(xp + ks * 32); }
  if (b < 96) {
    float a[8] = {0, 0, 0, 0, 0, 0, 0, 0};
#pragma unroll
    for (int i = 0; i < 4; ++i) {
      float c = lo16(cv[i]); c = rbf(c / (1.f + __expf(-c)));
#pragma unroll
      for (int e = 0; e < 4; ++e) { a[2 * e] += c * lo16(wv[i][e]); a[2 * e + 1] += c * hi16(wv[i][e]); }
    }
#pragma unroll
    for (int e = 0; e < 8; ++e) { float v = a[e]; v += ror<4>(v); v += ror<8>(v); a[e] = xsum(v); }
    if (lane < 4) { stsf4(L0 + L_SCR + wave * 128 + lane * 32, f32x4{a[0], a[1], a[2], a[3]}); stsf4(L0 + L_SCR + wave * 128 + lane * 32 + 16, f32x4{a[4], a[5], a[6], a[7]}); }
    bar();
    if (tid < 16) {
      float s0 = 0, s1 = 0;
#pragma unroll
      for (int w = 0; w < 8; ++w) { f32x2 v = *(LAS f32x2*)(L0 + L_SCR + w * 128 + tid * 8); s0 += v[0]; s1 += v[1]; }
      __hip_atomic_store((gu64*)(ws + WS_MOD) + b * 16 + tid, ((unsigned long long)tag << 32) | pk2(s0, s1), RLX_AGENT);
    }
  }
#pragma unroll
  for (int i = 0; i < 3; ++i) {
    int c0 = i * 512 + tid, row = c0 / 12, cc = c0 - row * 12, c = (cc >> 1) ^ ((row >> 3) & 1);
    const unsigned short* src = wqkv + row * 1536 + (c >> 1) * 512 + nt * 32 + (c & 1) * 16 + (cc & 1) * 8;
#pragma unroll
    for (int G = 0; G < 4; ++G) glds(src + G * 128 * 1536, wbase + (G * 1536 + i * 512) * 16);
  }
  const int pair0 = nt < 11 ? 3 * nt : 2 * nt + 11, npair = nt < 11 ? 3 : 2, pl = pair0 > 40 ? 40 : pair0;
  const unsigned short* gsrc[3];
#pragma unroll
  for (int i = 0; i < 3; ++i) {
    int c0 = i * 512 + tid, row = c0 / 24, cc = c0 - row * 24, c = (cc >> 1) ^ ((row & 2) | ((row >> 2) & 1));
    gsrc[i] = (c < 6 ? wg + c * 16 : wu + (c - 6) * 16) + row * 1376 + pl * 32 + (cc & 1) * 8;
  }
  auto load_gu = [&](int ks0, int slot0, int n2) {
#pragma unroll
    for (int G = 0; G < n2; ++G)
#pragma unroll
      for (int i = 0; i < 3; ++i) glds(gsrc[i] + (ks0 * 32 + G * 64) * 1376, wbase + slot0 * SLOT + (G * 1536 + i * 512) * 16);
  };
  const int off8 = (tid >> 3) * 512 + Nt * 64 + ((((tid & 7) >> 1) ^ (((tid >> 3) & 2) | ((tid >> 5) & 1))) * 16) + (tid & 1) * 8;
  s16x4 wp[2];
#pragma unroll
  for (int h = 0; h < 2; ++h)
#pragma unroll
    for (int e = 0; e < 4; ++e) wp[h][e] = (short)wphi[(nt * 2 + h) * 256 + (4 * g + e) * 16 + r16];

  float t[128];
  { float s = 0;
#pragma unroll
    for (int ks = 0; ks < 16; ++ks)
#pragma unroll
      for (int e = 0; e < 4; ++e) { t[ks * 8 + 2 * e] = lo16(xa[ks][e]); t[ks * 8 + 2 * e + 1] = hi16(xa[ks][e]); s += t[ks * 8 + 2 * e] + t[ks * 8 + 2 * e + 1]; }
    const float mean = xsum(s) * (1.f / 512.f); float v = 0;
#pragma unroll
    for (int i = 0; i < 128; ++i) { t[i] -= mean; v += t[i] * t[i]; }
    const float rstd = rsqrtf(xsum(v) * (1.f / 512.f) + 1e-5f);
#pragma unroll
    for (int i = 0; i < 128; ++i) t[i] *= rstd; }
  ST(1);
  { unsigned m = poll_granule(ws, tid, tag); float a = lo16(m), c = hi16(m);
    if (tid >= 256) { a = rbf(1.f + a); c = rbf(1.f + c); }
    *(LAS f32x2*)(L0 + L_MOD + tid * 8) = f32x2{a, c}; }
  wait_vm0(); bar();
  ST(2);
  load_gu(0, 8, 2);
  f32x4 acc[12];
#pragma unroll
  for (int c = 0; c < 12; ++c) acc[c] = f32x4{0, 0, 0, 0};
  { const unsigned be = L0 + (8 * g + q) * 192 + p * 8 + (g & 1) * 32, bo = be - (g & 1) * 64;
#pragma unroll
    for (int ks = 0; ks < 16; ++ks) {
      const unsigned ma = L0 + L_MOD + (ks * 32 + g * 8) * 4;
      f32x4 s0 = ldsf4(ma), s1 = ldsf4(ma + 16), c0 = ldsf4(ma + 2048), c1 = ldsf4(ma + 2064);
      const float* tt = t + ks * 8;
      u32x4 h = {pk2(tt[0] * c0[0] + s0[0], tt[1] * c0[1] + s0[1]), pk2(tt[2] * c0[2] + s0[2], tt[3] * c0[3] + s0[3]),
                 pk2(tt[4] * c1[0] + s1[0], tt[5] * c1[1] + s1[1]), pk2(tt[6] * c1[2] + s1[2], tt[7] * c1[3] + s1[3])};
#pragma unroll
      for (int c = 0; c < 6; ++c) {
        u32x4 w = frag(((c & 1) ? bo : be) + ks * 6144 + c * 32, 768);
        acc[c] = c < 4 ? mfma32(w, h, acc[c]) : mfma32(h, w, acc[c]);
      }
    } }
  ST(3);
  u32x4 phq[2];
  { f32x4 kv[4], zz[4]; const f32x4 Z = {0, 0, 0, 0}; const s16x4 ones = {0x3f80, 0x3f80, 0x3f80, 0x3f80};
#pragma unroll
    for (int h = 0; h < 2; ++h) {
      f32x4 pk = mfma16(pk4(acc[2 + h]), wp[h], Z), ep, en;
#pragma unroll
      for (int e = 0; e < 4; ++e) {
        float v = pk[e] * LOG2E, a = __builtin_amdgcn_exp2f(v - rmax(v)), c = __builtin_amdgcn_exp2f(rmin(v) - v);
        ep[e] = a * __builtin_amdgcn_rcpf(rsum(a)); en[e] = c * __builtin_amdgcn_rcpf(rsum(c));
      }
      s16x4 vb = pk4(acc[4 + h]), pp = pk4(ep), pn = pk4(en);
      kv[2 * h] = mfma16(pp, vb, Z); kv[2 * h + 1] = mfma16(pn, vb, Z);
      zz[2 * h] = mfma16(pp, ones, Z); zz[2 * h + 1] = mfma16(pn, ones, Z);
    }
    bar();
#pragma unroll
    for (int c = 0; c < 4; ++c) {
      stsf4(L0 + 65536 + wave * 4096 + c * 1024 + lane * 16, kv[c]);
      if (r16 == 0) stsf4(L0 + L_SCR + wave * 256 + c * 64 + g * 16, zz[c]);
    }
    bar();
    if (tid < 272) {
      const unsigned a = tid < 256 ? L0 + 65536 + tid * 16 : L0 + L_SCR + (tid - 256) * 16, st = tid < 256 ? 4096 : 256;
      f32x4 s = ldsf4(a);
#pragma unroll
      for (int w = 1; w < 8; ++w) s += ldsf4(a + w * st);
      __builtin_amdgcn_raw_buffer_store_b128(__builtin_bit_cast(u32x4, s), rs, WS_KV + (nt * 16 + mt) * 4352 + tid * 16, 0, 16);
    }
    publish(ws, mt * 16 + nt, tag);
    ST(4);
#pragma unroll
    for (int G = 0; G < 8; ++G) glds(wo + off8 + G * 64 * 512, wbase + G * 8192);
    load_gu(4, 6, 1);
#pragma unroll
    for (int h = 0; h < 2; ++h) {
      f32x4 pq = mfma16(wp[h], pk4(acc[h]), Z) * LOG2E, ep, en;
      const float mx = xmax(fmaxf(fmaxf(pq[0], pq[1]), fmaxf(pq[2], pq[3]))), mn = xmin(fminf(fminf(pq[0], pq[1]), fminf(pq[2], pq[3])));
#pragma unroll
      for (int e = 0; e < 4; ++e) { ep[e] = __builtin_amdgcn_exp2f(pq[e] - mx); en[e] = __builtin_amdgcn_exp2f(mn - pq[e]); }
      ep *= __builtin_amdgcn_rcpf(xsum(ep[0] + ep[1] + ep[2] + ep[3])); en *= __builtin_amdgcn_rcpf(xsum(en[0] + en[1] + en[2] + en[3]));
      phq[h] = u32x4{pk2(ep[0], ep[1]), pk2(ep[2], ep[3]), pk2(en[0], en[1]), pk2(en[2], en[3])};
    } }
#pragma unroll
  for (int i = 1; i < 3; ++i) { unsigned m = poll_granule(ws, i * 512 + tid, tag); float a = lo16(m), c = hi16(m);
    if (i == 2 && tid < 256) { a = rbf(1.f + a); c = rbf(1.f + c); }
    *(LAS f32x2*)(L0 + L_MOD + (i * 512 + tid) * 8) = f32x2{a, c}; }
  const int tok2 = Mt * 64 + wm * 16 + r16, col2 = Nt * 64 + wk * 32 + 4 * g;
  const u32x2 xr0 = *(const u32x2*)(x + tok2 * 512 + col2), xr1 = *(const u32x2*)(x + tok2 * 512 + col2 + 16);
  wait_flags(ws, nt, 16, tag, wave, lane);
  ST(5);
  { const unsigned kb = WS_KV + nt * 16 * 4352; u32x4 v[8], z;
#pragma unroll
    for (int i = 0; i < 8; ++i) v[i] = __builtin_amdgcn_raw_buffer_load_b128(rs, kb + (wk * 8 + i) * 4352 + (tid & 255) * 16, 0, 16);
    z = __builtin_amdgcn_raw_buffer_load_b128(rs, kb + ((tid >> 4) & 15) * 4352 + 4096 + r16 * 16, 0, 16);
    f32x4 s = __builtin_bit_cast(f32x4, v[0]), zf = __builtin_bit_cast(f32x4, z);
#pragma unroll
    for (int i = 1; i < 8; ++i) s += __builtin_bit_cast(f32x4, v[i]);
    stsf4(L0 + 65536 + tid * 16, s);
#pragma unroll
    for (int e = 0; e < 4; ++e) zf[e] = xsum(zf[e]);
    if (wk == 0 && g == 0) stsf4(L0 + L_SCR + 2048 + wave * 256 + r16 * 16, zf);
    bar();
    unsigned o[4];
#pragma unroll
    for (int h = 0; h < 2; ++h) {
      u32x4 ka, za;
#pragma unroll
      for (int c = 0; c < 2; ++c) {
        const unsigned a = L0 + 65536 + (2 * h + c) * 1024 + lane * 16, zb = L0 + L_SCR + 2048 + (2 * h + c) * 64 + g * 16;
        f32x4 k = ldsf4(a) + ldsf4(a + 4096), y = (ldsf4(zb) + ldsf4(zb + 256)) + (ldsf4(zb + 512) + ldsf4(zb + 768));
        ka[2 * c] = pk2(k[0], k[1]); ka[2 * c + 1] = pk2(k[2], k[3]); za[2 * c] = pk2(y[0], y[1]); za[2 * c + 1] = pk2(y[2], y[3]);
      }
      f32x4 num = mfma32(ka, phq[h], f32x4{0, 0, 0, 0}), den = mfma32(za, phq[h], f32x4{0, 0, 0, 0});
      const float rd = __builtin_amdgcn_rcpf(den[0] + 1e-6f);
      o[2 * h] = pk2(num[0] * rd, num[1] * rd); o[2 * h + 1] = pk2(num[2] * rd, num[3] * rd);
    }
    __builtin_amdgcn_raw_buffer_store_b128(u32x4{o[0], o[1], o[2], o[3]}, rs, WS_O + ((mt * 8 + wave) * 16 + nt) * 1024 + lane * 16, 0, 16);
    publish(ws, 256 + mt * 16 + nt, tag); }
  ST(6);
  const unsigned sw = (q & 2) | (g & 1);
  wait_flags(ws, 256 + (Mt >> 1) * 16, 1, tag, wave, lane);
  if (b == 0 && tid == 0) __hip_atomic_store((gu32*)ws, tag, RLX_AGENT);
  ST(7);
  unsigned x1p[4];
  { u32x4 a[8];
#pragma unroll
    for (int i = 0; i < 8; ++i) a[i] = __builtin_amdgcn_raw_buffer_load_b128(rs, WS_O + ((Mt * 4 + wm) * 16 + wk * 8 + i) * 1024 + lane * 16, 0, 16);
#pragma unroll
    for (int c = 0; c < 4; ++c) acc[c] = f32x4{0, 0, 0, 0};
    const unsigned wb = L0 + wk * 32768 + (4 * g + q) * 128 + p * 8;
#pragma unroll
    for (int i = 0; i < 8; ++i)
#pragma unroll
      for (int c = 0; c < 4; ++c) acc[c] = mfma32(frag(wb + ((c ^ sw) << 5) + i * 4096, 2048), a[i], acc[c]);
    bar();
    stsf4(L0 + wave * 2048 + lane * 16, wk ? acc[0] : acc[2]); stsf4(L0 + wave * 2048 + 1024 + lane * 16, wk ? acc[1] : acc[3]);
    bar();
    f32x4 r0 = (wk ? acc[2] : acc[0]) + ldsf4(L0 + (wave ^ 4) * 2048 + lane * 16), r1 = (wk ? acc[3] : acc[1]) + ldsf4(L0 + (wave ^ 4) * 2048 + 1024 + lane * 16);
    const f32x4 g0 = ldsf4(L0 + L_MOD + (1024 + col2) * 4), g1 = ldsf4(L0 + L_MOD + (1024 + col2 + 16) * 4);
    x1p[0] = pk2(lo16(xr0[0]) + g0[0] * r0[0], hi16(xr0[0]) + g0[1] * r0[1]); x1p[1] = pk2(lo16(xr0[1]) + g0[2] * r0[2], hi16(xr0[1]) + g0[3] * r0[3]);
    x1p[2] = pk2(lo16(xr1[0]) + g1[0] * r1[0], hi16(xr1[0]) + g1[1] * r1[1]); x1p[3] = pk2(lo16(xr1[1]) + g1[2] * r1[2], hi16(xr1[1]) + g1[3] * r1[3]);
    __builtin_amdgcn_raw_buffer_store_b128(u32x4{x1p[0], x1p[1], x1p[2], x1p[3]}, rs, WS_X1 + ((Mt * 4 + wm) * 16 + Nt * 2 + wk) * 1024 + lane * 16, 0, 16);
    float s = 0, v = 0;
#pragma unroll
    for (int e = 0; e < 4; ++e) s += lo16(x1p[e]) + hi16(x1p[e]);
    s = xsum(s) * (1.f / 32.f);
#pragma unroll
    for (int e = 0; e < 4; ++e) { float d0 = lo16(x1p[e]) - s, d1 = hi16(x1p[e]) - s; v += d0 * d0 + d1 * d1; }
    v = xsum(v);
    if (g == 0) __builtin_amdgcn_raw_buffer_store_b64(u32x2{__float_as_uint(s), __float_as_uint(v)}, rs, WS_ST + tok2 * 128 + (Nt * 2 + wk) * 8, 0, 16);
    publish(ws, 512 + Mt * 8 + Nt, tag); }
  ST(8);
  load_gu(6, 0, 3);
  wait_vm0();
  wait_flags(ws, 512 + mt * 16, 1, tag, wave, lane);
  ST(9);
  { const unsigned sb = WS_ST + (mt * 128 + wave * 16 + r16) * 128 + g * 32;
    f32x4 s0 = __builtin_bit_cast(f32x4, __builtin_amdgcn_raw_buffer_load_b128(rs, sb, 0, 16));
    f32x4 s1 = __builtin_bit_cast(f32x4, __builtin_amdgcn_raw_buffer_load_b128(rs, sb + 16, 0, 16));
#pragma unroll
    for (int ks = 0; ks < 16; ++ks) xa[ks] = __builtin_amdgcn_raw_buffer_load_b128(rs, WS_X1 + ((mt * 8 + wave) * 16 + ks) * 1024 + lane * 16, 0, 16);
    const float mean = xsum((s0[0] + s0[2]) + (s1[0] + s1[2])) * (1.f / 16.f);
    const float d0 = s0[0] - mean, d1 = s0[2] - mean, d2 = s1[0] - mean, d3 = s1[2] - mean;
    const float var = xsum((s0[1] + s0[3]) + (s1[1] + s1[3]) + 32.f * (d0 * d0 + d1 * d1 + d2 * d2 + d3 * d3)) * (1.f / 512.f);
    const float rstd = rsqrtf(var + 1e-5f), nmr = -mean * rstd;
#pragma unroll
    for (int c = 0; c < 12; ++c) acc[c] = f32x4{0, 0, 0, 0};
    const unsigned wb = L0 + (4 * g + q) * 384 + p * 8;
#pragma unroll
    for (int ks = 0; ks < 16; ++ks) {
      if (ks == 4) { bar(); load_gu(12, 8, 2); }
      if (ks == 12) { wait_vm0(); bar();
#pragma unroll
        for (int G = 0; G < 12; ++G) glds(wd + off8 + ((G < 6 ? 0 : 22 * 32 - 6 * 64) + G * 64) * 512, wbase + G * 8192); }
      const int slot = ks < 4 ? 8 + ks : ks < 6 ? ks + 2 : ks < 12 ? ks - 6 : ks - 4;
      const unsigned ma = L0 + L_MOD + (1536 + ks * 32 + g * 4) * 4;
      f32x4 b0 = ldsf4(ma), b1 = ldsf4(ma + 64), c0 = ldsf4(ma + 2048), c1 = ldsf4(ma + 2048 + 64);
      u32x4 h;
#pragma unroll
      for (int e = 0; e < 2; ++e) {
        h[e] = pk2((lo16(xa[ks][e]) * rstd + nmr) * c0[2 * e] + b0[2 * e], (hi16(xa[ks][e]) * rstd + nmr) * c0[2 * e + 1] + b0[2 * e + 1]);
        h[2 + e] = pk2((lo16(xa[ks][2 + e]) * rstd + nmr) * c1[2 * e] + b1[2 * e], (hi16(xa[ks][2 + e]) * rstd + nmr) * c1[2 * e + 1] + b1[2 * e + 1]);
      }
#pragma unroll
      for (int c = 0; c < 12; ++c) acc[c] = mfma32(frag(wb + (((c & 3) ^ sw) << 5) + slot * SLOT + (c >> 2) * 128, 6144), h, acc[c]);
    }
    ST(10);
#pragma unroll
    for (int jp = 0; jp < 3; ++jp) {
      u32x4 hv;
#pragma unroll
      for (int u = 0; u < 2; ++u) {
        f32x4 gg = acc[2 * jp + u], uu = acc[6 + 2 * jp + u], r;
#pragma unroll
        for (int e = 0; e < 4; ++e) r[e] = uu[e] * __builtin_amdgcn_rcpf(1.f + __builtin_amdgcn_exp2f(-LOG2E * gg[e]));
        hv[2 * u] = pk2(r[0], r[1]); hv[2 * u + 1] = pk2(r[2], r[3]);
      }
      if (pl + jp >= pair0 && pl + jp < pair0 + npair)
        __builtin_amdgcn_raw_buffer_store_b128(hv, rs, WS_H + ((mt * 8 + wave) * 43 + pl + jp) * 1024 + lane * 16, 0, 16);
    }
    publish(ws, 768 + mt * 16 + nt, tag); }
  ST(11);
#pragma unroll
  for (int G = 0; G < 6; ++G) glds(wd + off8 + ((G < 3 ? 12 * 32 : 34 * 32 - 3 * 64) + G * 64) * 512, wbase + 98304 + G * 8192);
  wait_vm0();
  wait_flags(ws, 768 + (Mt >> 1) * 16, 1, tag, wave, lane);
  ST(12);
  { u32x4 a[22];
    const unsigned hb = WS_H + ((Mt * 4 + wm) * 43 + wk * 22) * 1024 + lane * 16;
#pragma unroll
    for (int i = 0; i < 21; ++i) a[i] = __builtin_amdgcn_raw_buffer_load_b128(rs, hb + i * 1024, 0, 16);
    a[21] = __builtin_amdgcn_raw_buffer_load_b128(rs, hb + (21 - wk) * 1024, 0, 16);
#pragma unroll
    for (int c = 0; c < 4; ++c) acc[c] = f32x4{0, 0, 0, 0};
    const unsigned wb = L0 + (4 * g + q) * 128 + p * 8, wbA = wb + wk * 49152, wbB = wb + 98304 + wk * 24576;
#pragma unroll
    for (int i = 0; i < 22; ++i) {
      if (i == 4) { bar();
        glds(wd + off8 + 18 * 32 * 512, wbase); glds(wd + off8 + 20 * 32 * 512, wbase + 8192); glds(wd + off8 + 40 * 32 * 512, wbase + 49152);
        if (wave < 4) glds(wd + off8 + 42 * 32 * 512, wbase + 49152 + 8192); }
      if (i == 18) { wait_vm0(); bar(); }
      if (i < 21 || wk == 0) {
#pragma unroll
        for (int c = 0; c < 4; ++c)
          acc[c] = mfma32(frag((i < 12 ? wbA + i * 4096 : i < 18 ? wbB + (i - 12) * 4096 : wbA + (i - 18) * 4096) + ((c ^ sw) << 5), 2048), a[i], acc[c]);
      }
    }
    ST(13);
    stsf4(L0 + 98304 + wave * 2048 + lane * 16, wk ? acc[0] : acc[2]); stsf4(L0 + 98304 + wave * 2048 + 1024 + lane * 16, wk ? acc[1] : acc[3]);
    bar();
    f32x4 r0 = (wk ? acc[2] : acc[0]) + ldsf4(L0 + 98304 + (wave ^ 4) * 2048 + lane * 16), r1 = (wk ? acc[3] : acc[1]) + ldsf4(L0 + 98304 + (wave ^ 4) * 2048 + 1024 + lane * 16);
    const f32x4 g0 = ldsf4(L0 + L_MOD + (2560 + col2) * 4), g1 = ldsf4(L0 + L_MOD + (2560 + col2 + 16) * 4);
    *(u32x2*)(out + tok2 * 512 + col2) = u32x2{pk2(lo16(x1p[0]) + g0[0] * r0[0], hi16(x1p[0]) + g0[1] * r0[1]), pk2(lo16(x1p[1]) + g0[2] * r0[2], hi16(x1p[1]) + g0[3] * r0[3])};
    *(u32x2*)(out + tok2 * 512 + col2 + 16) = u32x2{pk2(lo16(x1p[2]) + g1[0] * r1[0], hi16(x1p[2]) + g1[1] * r1[1]), pk2(lo16(x1p[3]) + g1[2] * r1[2], hi16(x1p[3]) + g1[3] * r1[3])};
  }
  ST(14);
}

extern "C" void kernel_launch(void* const* d_in, const int* in_sizes, int n_in, void* d_out, int out_size, void* d_ws, size_t ws_size,
                              hipStream_t stream) {
  typedef const unsigned short* P;
  adaln_block<<<256, 512, 0, stream>>>((P)d_in[0], (P)d_in[1], (P)d_in[2], (P)d_in[3], (P)d_in[5], (P)d_in[6], (P)d_in[7], (P)d_in[8], (P)d_in[9],
                                       (unsigned short*)d_out, (char*)d_ws);
}
```

```cpp
#include <hip/hip_runtime.h>
#include <hip/hip_bf16.h>

typedef __attribute__((ext_vector_type(8))) __bf16 bf16x8;
typedef __attribute__((ext_vector_type(2))) __bf16 bf16x2;
typedef __attribute__((ext_vector_type(4))) short s16x4;
typedef __attribute__((ext_vector_type(4))) float f32x4;
typedef __attribute__((ext_vector_type(2))) float f32x2;
typedef __attribute__((ext_vector_type(16))) float f32x16;
typedef __attribute__((ext_vector_type(4))) unsigned u32x4;
typedef __attribute__((ext_vector_type(2))) unsigned u32x2;
typedef __attribute__((address_space(1))) unsigned gu32;
typedef __attribute__((address_space(1))) unsigned long long gu64;
#define LAS __attribute__((address_space(3)))
#define RLX_AGENT __ATOMIC_RELAXED, __HIP_MEMORY_SCOPE_AGENT
#define DEV __device__ __forceinline__

constexpr unsigned WS_FLAG = 4096;
constexpr unsigned WS_MOD = WS_FLAG + 4 * 256 * 128;
constexpr unsigned WS_KV = WS_MOD + 16384;
constexpr unsigned WS_ST = WS_KV + 256 * 4352;
constexpr unsigned WS_O = WS_ST + 2048 * 128;
constexpr unsigned WS_X1 = WS_O + 2097152;
constexpr unsigned WS_H = WS_X1 + 2097152;
constexpr unsigned WS_END = WS_H + 128 * 43 * 1024;
constexpr unsigned L_MOD = 147456, L_SCR = 159744;
constexpr unsigned SPIN_CAP = 1u << 19;

DEV unsigned pk2(float a, float b) { f32x2 v = {a, b}; return __builtin_bit_cast(unsigned, __builtin_convertvector(v, bf16x2)); }
DEV float lo16(unsigned u) { return __uint_as_float(u << 16); }
DEV float hi16(unsigned u) { return __uint_as_float(u & 0xffff0000u); }
DEV float rbf(float a) { return lo16(pk2(a, a)); }
DEV f32x16 mfma(u32x4 a, u32x4 b, f32x16 c) {
  return __builtin_amdgcn_mfma_f32_32x32x16_bf16(__builtin_bit_cast(bf16x8, a), __builtin_bit_cast(bf16x8, b), c, 0, 0, 0); }
DEV u32x4 pk8(const float* v) { return u32x4{pk2(v[0], v[1]), pk2(v[2], v[3]), pk2(v[4], v[5]), pk2(v[6], v[7])}; }
DEV void swap32(unsigned& a, unsigned& b) { auto s = __builtin_amdgcn_permlane32_swap(a, b, false, false); a = s[0]; b = s[1]; }
DEV float hsum(float x) { unsigned a = __float_as_uint(x), b = a; swap32(a, b); return __uint_as_float(a) + __uint_as_float(b); }
DEV float hmax(float x) { unsigned a = __float_as_uint(x), b = a; swap32(a, b); return fmaxf(__uint_as_float(a), __uint_as_float(b)); }
#define QSEL(o0, o1, mv0, keep0, mv1, keep1, MASK, QP) asm("s_mov_b32 vcc_lo, " MASK "\n\ts_mov_b32 vcc_hi, " MASK "\n\ts_nop 1\n\t" \
  "v_cndmask_b32_dpp %0, %8, %16, vcc " QP "\n\tv_cndmask_b32_dpp %1, %9, %17, vcc " QP "\n\tv_cndmask_b32_dpp %2, %10, %18, vcc " QP "\n\tv_cndmask_b32_dpp %3, %11, %19, vcc " QP "\n\t" \
  "v_cndmask_b32_dpp %4, %12, %20, vcc " QP "\n\tv_cndmask_b32_dpp %5, %13, %21, vcc " QP "\n\tv_cndmask_b32_dpp %6, %14, %22, vcc " QP "\n\tv_cndmask_b32_dpp %7, %15, %23, vcc " QP \
  : "=&v"(o0[0]), "=&v"(o0[1]), "=&v"(o0[2]), "=&v"(o0[3]), "=&v"(o1[0]), "=&v"(o1[1]), "=&v"(o1[2]), "=&v"(o1[3]) \
  : "v"(mv0[0]), "v"(mv0[1]), "v"(mv0[2]), "v"(mv0[3]), "v"(mv1[0]), "v"(mv1[1]), "v"(mv1[2]), "v"(mv1[3]), \
    "v"(keep0[0]), "v"(keep0[1]), "v"(keep0[2]), "v"(keep0[3]), "v"(keep1[0]), "v"(keep1[1]), "v"(keep1[2]), "v"(keep1[3]) : "vcc")
#define QX1 "quad_perm:[1,0,3,2] row_mask:0xf bank_mask:0xf"
#define QX2 "quad_perm:[2,3,0,1] row_mask:0xf bank_mask:0xf"
#define XRED(name, OP) DEV float name(float x) { \
  auto s = __builtin_amdgcn_permlane16_swap(__float_as_uint(x), __float_as_uint(x), false, false); \
  x = OP(__uint_as_float(s[0]), __uint_as_float(s[1])); \
  auto t = __builtin_amdgcn_permlane32_swap(__float_as_uint(x), __float_as_uint(x), false, false); \
  return OP(__uint_as_float(t[0]), __uint_as_float(t[1])); }
DEV float addf(float a, float b) { return a + b; }
XRED(xsum, addf)
template <int N> DEV float ror(float x) { return __int_as_float(__builtin_amdgcn_update_dpp(0, __float_as_int(x), 0x120 + N, 0xf, 0xf, false)); }

DEV void glds(const void* g, unsigned lds) {
  unsigned keep;
  asm volatile("s_mov_b32 %0, m0\n\ts_mov_b32 m0, %2\n\ts_nop 0\n\tglobal_load_lds_dwordx4 %1, off\n\ts_mov_b32 m0, %0"
               : "=&s"(keep) : "v"(g), "s"(lds) : "memory");
}
DEV void wait_vm0() { asm volatile("s_waitcnt vmcnt(0)" ::: "memory"); }
DEV void bar() { asm volatile("s_waitcnt lgkmcnt(0)\n\ts_barrier" ::: "memory"); }
typedef LAS char* lp;
DEV lp opq(unsigned a) { asm volatile("" : "+v"(a)); return (lp)a; }
DEV u32x4 fragp(lp a, int o, int d) {
  u32x2 x = __builtin_bit_cast(u32x2, __builtin_amdgcn_ds_read_tr16_b64_v4i16((LAS s16x4*)(a + o)));
  u32x2 y = __builtin_bit_cast(u32x2, __builtin_amdgcn_ds_read_tr16_b64_v4i16((LAS s16x4*)(a + o + d)));
  return u32x4{x[0], x[1], y[0], y[1]}; }
DEV f32x4 ldp4(lp a, int o) { return *(LAS f32x4*)(a + o); }
#define SB0() __builtin_amdgcn_sched_barrier(0)
template <int NM, int ND, int NV> DEV void ilv() {
#pragma unroll
  for (int i = 0; i < NM; ++i) { __builtin_amdgcn_sched_group_barrier(0x008, 1, 0); __builtin_amdgcn_sched_group_barrier(0x100, ND, 0);
    if (NV) __builtin_amdgcn_sched_group_barrier(0x002, NV, 0); }
}
DEV f32x4 ldsf4(unsigned a) { return *(LAS f32x4*)a; }
DEV void stsf4(unsigned a, f32x4 v) { *(LAS f32x4*)a = v; }

DEV void wait_flags(char* ws, unsigned first, unsigned stride, unsigned n, unsigned tag, int wave, int lane) {
  if (wave == 0) {
    gu32* p = (gu32*)(ws + WS_FLAG) + (first + (lane & (n - 1)) * stride) * 32;
    for (unsigned s = 0; s < SPIN_CAP; ++s) {
      if (__all(__hip_atomic_load(p, RLX_AGENT) == tag)) break;
      __builtin_amdgcn_s_sleep(1);
    }
  }
  bar();
}
DEV void publish(char* ws, unsigned idx, unsigned tag) {
  wait_vm0(); bar();
  if (threadIdx.x == 0) __hip_atomic_store((gu32*)(ws + WS_FLAG) + idx * 32, tag, RLX_AGENT);
}
DEV unsigned poll_granule(char* ws, unsigned idx, unsigned tag) {
  gu64* p = (gu64*)(ws + WS_MOD) + idx; unsigned long long v = 0;
  for (unsigned s = 0; s < SPIN_CAP; ++s) {
    v = __hip_atomic_load(p, RLX_AGENT);
    if (__all((unsigned)(v >> 32) == tag)) break;
    __builtin_amdgcn_s_sleep(1);
  }
  return (unsigned)v;
}
#define ST(i)
#define ST2(i)

DEV void hedgehog(u32x4 wp, const float* v, u32x4& o0, u32x4& o1) {
  const float L = 1.4426950408889634f; f32x16 Z = {0}; f32x16 pp = mfma(wp, pk8(v), Z); float e[16];
#pragma unroll
  for (int s = 0; s < 2; ++s) {
    float m = fmaxf(fmaxf(fmaxf(pp[8 * s], pp[8 * s + 1]), fmaxf(pp[8 * s + 2], pp[8 * s + 3])), fmaxf(fmaxf(pp[8 * s + 4], pp[8 * s + 5]), fmaxf(pp[8 * s + 6], pp[8 * s + 7])));
    m = -hmax(m) * L; float t = 0;
#pragma unroll
    for (int i = 0; i < 8; ++i) { e[8 * s + i] = __builtin_amdgcn_exp2f(pp[8 * s + i] * L + m); t += e[8 * s + i]; }
    t = __builtin_amdgcn_rcpf(hsum(t));
#pragma unroll
    for (int i = 0; i < 8; ++i) e[8 * s + i] *= t;
  }
  o0 = pk8(e); o1 = pk8(e + 8);
}
DEV void reduce4(unsigned base, const f32x16 (&acc)[3], int wave, int lane, float* r) {
  const int wm = wave & 1, wq = wave >> 1;
#pragma unroll
  for (int d = 0; d < 4; ++d) {
    const f32x16 v = acc[d >> 1]; const int o = (d & 1) * 8;
    if (d != wq) { const unsigned a = base + ((d * 2 + wm) * 3 + ((wq - d - 1) & 3)) * 2048 + lane * 16;
      stsf4(a, f32x4{v[o], v[o + 1], v[o + 2], v[o + 3]}); stsf4(a + 1024, f32x4{v[o + 4], v[o + 5], v[o + 6], v[o + 7]}); }
    else {
#pragma unroll
      for (int i = 0; i < 8; ++i) r[i] = v[o + i]; }
  }
  bar();
#pragma unroll
  for (int s = 0; s < 6; ++s) { const f32x4 v = ldsf4(base + wave * 6144 + s * 1024 + lane * 16);
#pragma unroll
    for (int i = 0; i < 4; ++i) r[(s & 1) * 4 + i] += v[i]; }
}

__global__ void __launch_bounds__(512, 2)
adaln_block(const unsigned short* __restrict__ x, const unsigned short* __restrict__ wqkv, const unsigned short* __restrict__ wphi,
            const unsigned short* __restrict__ wo, const unsigned short* __restrict__ wg, const unsigned short* __restrict__ wu,
            const unsigned short* __restrict__ wd, const unsigned short* __restrict__ cin, const unsigned short* __restrict__ wmod,
            unsigned short* __restrict__ out, char* ws) {
  __shared__ __attribute__((aligned(16))) char smem[163840];
  const unsigned L0 = (unsigned)(unsigned long long)(LAS char*)smem;
  const int tid = threadIdx.x, lane = tid & 63, wave = __builtin_amdgcn_readfirstlane(tid >> 6);
  const int h = lane >> 5, r32 = lane & 31, cb = (lane >> 4) & 1, q = (lane & 15) >> 2, p = lane & 3;
  const int b = blockIdx.x, xc = b & 7, j = b >> 3;
  const int mt = (xc >> 1) * 4 + (j & 3), nt = (xc & 1) * 8 + (j >> 2);
  const int Mt = (xc >> 1) * 8 + (j & 7), Nt = (xc & 1) * 4 + (j >> 3);
  const int m3 = (xc >> 1) * 2 + (j & 1), nf = (xc & 1) * 16 + (j >> 1);
  const int wm = wave & 3, wk = wave >> 2, wm2 = wave & 1, wq = wave >> 1;
  const __amdgpu_buffer_rsrc_t rs = __builtin_amdgcn_make_buffer_rsrc(ws, 0, WS_END, 0x00020000);
  const unsigned wbase = L0 + wave * 1024;
  const f32x16 Z16 = {0};
  ST(0);
  const unsigned tag = __builtin_amdgcn_readfirstlane(__hip_atomic_load((gu32*)ws, RLX_AGENT)) + 1u;

  u32x4 wv[4]; unsigned short cv[4];
  if (b < 96) {
#pragma unroll
    for (int i = 0; i < 4; ++i) { int row = (tid >> 2) + 128 * i; wv[i] = *(const u32x4*)(wmod + row * 3072 + b * 32 + (tid & 3) * 8); cv[i] = cin[row]; }
  }
  u32x4 xa[32];
  { const unsigned short* xp = x + (mt * 128 + wm * 32 + (r32 & 28)) * 512 + wk * 256 + h * 128 + p * 8;
#pragma unroll
    for (int i = 0; i < 16; ++i) xa[i] = *(const u32x4*)(xp + (i & 3) * 512 + (i >> 2) * 32); }
  if (b < 96) {
    float a[8] = {0, 0, 0, 0, 0, 0, 0, 0};
#pragma unroll
    for (int i = 0; i < 4; ++i) {
      float c = lo16(cv[i]); c = rbf(c / (1.f + __expf(-c)));
#pragma unroll
      for (int e = 0; e < 4; ++e) { a[2 * e] += c * lo16(wv[i][e]); a[2 * e + 1] += c * hi16(wv[i][e]); }
    }
#pragma unroll
    for (int e = 0; e < 8; ++e) { float v = a[e]; v += ror<4>(v); v += ror<8>(v); a[e] = xsum(v); }
    if (lane < 4) { stsf4(L0 + L_SCR + wave * 128 + lane * 32, f32x4{a[0], a[1], a[2], a[3]}); stsf4(L0 + L_SCR + wave * 128 + lane * 32 + 16, f32x4{a[4], a[5], a[6], a[7]}); }
    bar();
    if (tid < 16) {
      float s0 = 0, s1 = 0;
#pragma unroll
      for (int w = 0; w < 8; ++w) { f32x2 v = *(LAS f32x2*)(L0 + L_SCR + w * 128 + tid * 8); s0 += v[0]; s1 += v[1]; }
      __hip_atomic_store((gu64*)(ws + WS_MOD) + b * 16 + tid, ((unsigned long long)tag << 32) | pk2(s0, s1), RLX_AGENT);
    }
  }
  u32x4 wp;
  { const unsigned short* pp = wphi + (nt * 2 + wk) * 256 + 4 * h * 16 + (lane & 15); const unsigned sg = cb ? 0x80008000u : 0u;
#pragma unroll
    for (int e = 0; e < 4; ++e) { const int d = (e >> 1) * 8 + (e & 1) * 2; wp[e] = ((unsigned)pp[d * 16] | ((unsigned)pp[d * 16 + 16] << 16)) ^ sg; } }
  const int u0 = nf < 22 ? 3 * nf : 2 * nf + 22, nu = nf < 22 ? 3 : 2, ul = u0 > 83 ? 83 : u0;
  const unsigned short* gsrc[3];
#pragma unroll
  for (int i = 0; i < 3; ++i) {
    const int c0 = i * 512 + tid, row = c0 / 12, cc = c0 - row * 12;
    const unsigned short* src = wqkv + row * 1536 + (cc >> 2) * 512 + nt * 32 + (cc & 3) * 8;
#pragma unroll
    for (int G = 0; G < 4; ++G) glds(src + G * 128 * 1536, wbase + (G * 3 + i) * 8192);
    gsrc[i] = ((cc & 2) ? wu : wg) + row * 1376 + (ul + (cc >> 2)) * 16 + (cc & 1) * 8;
  }
  auto load_gu = [&](int n0, int n1) {
#pragma unroll
    for (int n = n0; n < n1; ++n) glds(gsrc[n % 3] + (n / 3) * 128 * 1376, wbase + 49152 + n * 8192); };
  const int off8 = (wm2 * 8 + (lane >> 3)) * 512 + Nt * 64 + ((((lane >> 2) ^ (lane >> 4)) & 1) * 32) + p * 8;
  const int ks4 = wq * 22 - (wq == 3);
  auto load_wd = [&](int i0, int i1) {
#pragma unroll
    for (int i = i0; i < i1; ++i) glds(wd + off8 + (i == 21 && wq == 3 ? 85 : ks4 + i) * 8192, wbase + (i < 18 ? i : i - 18) * 8192); };

  f32x2 t[64];
  { f32x2 sv = {0, 0}, vv = {0, 0};
#pragma unroll
    for (int s = 0; s < 4; ++s) { unsigned n[4][4], m[4][4]; const u32x4 r0 = xa[4 * s], r1 = xa[4 * s + 1], r2 = xa[4 * s + 2], r3 = xa[4 * s + 3];
      QSEL(n[1], n[3], r0, r1, r2, r3, "0xaaaaaaaa", QX1); QSEL(n[0], n[2], r1, r0, r3, r2, "0x55555555", QX1);
      QSEL(m[2], m[3], n[0], n[2], n[1], n[3], "0xcccccccc", QX2); QSEL(m[0], m[1], n[2], n[0], n[3], n[1], "0x33333333", QX2);
#pragma unroll
      for (int i = 0; i < 16; ++i) { t[16 * s + i] = f32x2{lo16(m[i >> 2][i & 3]), hi16(m[i >> 2][i & 3])}; sv += t[16 * s + i]; } }
    const float mh = hsum(sv[0] + sv[1]) * (1.f / 256.f);
#pragma unroll
    for (int i = 0; i < 64; ++i) { t[i] -= mh; vv += t[i] * t[i]; }
    const float vh = hsum(vv[0] + vv[1]);
    if (h == 0) *(LAS f32x2*)(L0 + L_SCR + 1024 + wave * 256 + r32 * 8) = f32x2{mh, vh};
    bar();
    const f32x2 o = *(LAS f32x2*)(L0 + L_SCR + 1024 + (wave ^ 4) * 256 + r32 * 8);
    const float dm = mh - o[0], rstd = rsqrtf((vh + o[1] + 128.f * dm * dm) * (1.f / 512.f) + 1e-5f), dl = 0.5f * dm * rstd;
#pragma unroll
    for (int i = 0; i < 64; ++i) t[i] = t[i] * rstd + dl; }
  ST(1);
  { unsigned m = poll_granule(ws, tid, tag); float a = lo16(m), c = hi16(m);
    if (tid >= 256) { a = rbf(1.f + a); c = rbf(1.f + c); }
    *(LAS f32x2*)(L0 + L_MOD + tid * 8) = f32x2{a, c}; }
  wait_vm0(); bar();
  ST(2);
  f32x16 acc[3] = {Z16, Z16, Z16};
  { const lp wb = opq(L0 + (wk * 256 + h * 128 + q) * 192 + cb * 32 + p * 8), mp = opq(L0 + L_MOD + (wk * 256 + h * 128) * 4);
    u32x4 w[2][3], hh[2]; f32x4 m[4];
    auto ldw = [&](int st) {
#pragma unroll
      for (int c = 0; c < 3; ++c) w[st & 1][c] = fragp(wb, st * 1536 + c * 64, 768); };
    auto ldm = [&](int st) { m[0] = ldp4(mp, st * 32); m[1] = ldp4(mp, st * 32 + 16); m[2] = ldp4(mp, st * 32 + 2048); m[3] = ldp4(mp, st * 32 + 2064); };
    auto mkh = [&](int st) {
#pragma unroll
      for (int e = 0; e < 4; ++e) { const int o = (e & 1) * 2; const f32x4 sa = m[e >> 1], ca = m[2 + (e >> 1)];
        const f32x2 r = t[st * 4 + e] * f32x2{ca[o], ca[o + 1]} + f32x2{sa[o], sa[o + 1]}; hh[st & 1][e] = pk2(r[0], r[1]); } };
    ldm(0); ldw(0); mkh(0); ldm(1); SB0();
#pragma unroll
    for (int st = 0; st < 16; ++st) {
      acc[0] = mfma(w[st & 1][0], hh[st & 1], acc[0]); SB0();
      if (st < 15) mkh(st + 1);
      SB0(); acc[1] = mfma(w[st & 1][1], hh[st & 1], acc[1]); SB0();
      if (st < 15) ldw(st + 1);
      SB0(); acc[2] = mfma(w[st & 1][2], hh[st & 1], acc[2]); SB0();
      if (st < 14) ldm(st + 2);
      SB0();
    } }
  ST(3);
  u32x4 phq0, phq1;
  { float v[3][8];
#pragma unroll
    for (int T = 0; T < 3; ++T) { float s[8];
#pragma unroll
      for (int i = 0; i < 8; ++i) { s[i] = wk ? acc[T][i] : acc[T][8 + i]; v[T][i] = wk ? acc[T][8 + i] : acc[T][i]; }
      stsf4(L0 + 98304 + wave * 6144 + T * 2048 + lane * 16, f32x4{s[0], s[1], s[2], s[3]}); stsf4(L0 + 98304 + wave * 6144 + T * 2048 + 1024 + lane * 16, f32x4{s[4], s[5], s[6], s[7]}); }
    bar();
#pragma unroll
    for (int i = 0; i < 8; ++i) glds(wo + off8 + (wq * 8 + i) * 8192, wbase + i * 8192);
#pragma unroll
    for (int s = 0; s < 6; ++s) { const f32x4 o = ldsf4(L0 + 98304 + (wave ^ 4) * 6144 + s * 1024 + lane * 16);
#pragma unroll
      for (int i = 0; i < 4; ++i) v[s >> 1][(s & 1) * 4 + i] += o[i]; }
    u32x4 k0, k1; hedgehog(wp, v[1], k0, k1);
    const u32x4 vb = pk8(v[2]);
    const unsigned img = L0 + 65536 + wave * 4096, wr = img + r32 * 104 + h * 8;
    *(LAS u32x2*)(wr) = u32x2{k0[0], k0[1]}; *(LAS u32x2*)(wr + 16) = u32x2{k0[2], k0[3]}; *(LAS u32x2*)(wr + 32) = u32x2{k1[0], k1[1]}; *(LAS u32x2*)(wr + 48) = u32x2{k1[2], k1[3]};
    *(LAS u32x2*)(wr + 64) = u32x2{vb[0], vb[1]}; *(LAS u32x2*)(wr + 80) = u32x2{vb[2], vb[3]};
    asm volatile("" ::: "memory");
    f32x16 kv = Z16; const lp ra = opq(img + (8 * h + q) * 104 + cb * 32 + p * 8), rb = opq(img + (8 * h + q) * 104 + 64 + p * 8);
#pragma unroll
    for (int s = 0; s < 2; ++s) { u32x4 bb = fragp(rb, s * 1664, 416);
#pragma unroll
      for (int e = 0; e < 4; ++e) bb[e] = cb ? 0x3f803f80u : bb[e];
      kv = mfma(fragp(ra, s * 1664, 416), bb, kv); }
    asm volatile("" ::: "memory");
    if (r32 <= 16) {
#pragma unroll
      for (int c = 0; c < 4; ++c) stsf4(img + (c * 34 + h * 17 + r32) * 16, f32x4{kv[4 * c], kv[4 * c + 1], kv[4 * c + 2], kv[4 * c + 3]}); }
    bar();
    if (tid < 272) { const unsigned a = L0 + 65536 + (tid < 136 ? tid * 16 : 16384 + (tid - 136) * 16);
      const f32x4 s = (ldsf4(a) + ldsf4(a + 4096)) + (ldsf4(a + 8192) + ldsf4(a + 12288));
      __builtin_amdgcn_raw_buffer_store_b128(__builtin_bit_cast(u32x4, s), rs, WS_KV + (nt * 16 + mt) * 4352 + tid * 16, 0, 16); }
    publish(ws, mt * 16 + nt, tag);
    ST(4);
    hedgehog(wp, v[0], phq0, phq1); }
#pragma unroll
  for (int i = 1; i < 3; ++i) { unsigned m = poll_granule(ws, i * 512 + tid, tag); float a = lo16(m), c = hi16(m);
    if (i == 2 && tid < 256) { a = rbf(1.f + a); c = rbf(1.f + c); }
    *(LAS f32x2*)(L0 + L_MOD + (i * 512 + tid) * 8) = f32x2{a, c}; }
  const int tok2 = Mt * 64 + wm2 * 32 + r32, col2 = Nt * 64 + wq * 16;
  const u32x4 xr = *(const u32x4*)(x + tok2 * 512 + col2 + 8 * h);
  load_gu(2, 7);
  wait_flags(ws, nt, 16, 16, tag, wave, lane);
  ST(5);
  { if (tid < 272) { u32x4 v[16];
#pragma unroll
      for (int i = 0; i < 16; ++i) v[i] = __builtin_amdgcn_raw_buffer_load_b128(rs, WS_KV + (nt * 16 + i) * 4352 + tid * 16, 0, 16);
      f32x4 s = __builtin_bit_cast(f32x4, v[0]);
#pragma unroll
      for (int i = 1; i < 16; ++i) s += __builtin_bit_cast(f32x4, v[i]);
      *(LAS u32x2*)(L0 + L_SCR + tid * 8) = u32x2{pk2(s[0], s[1]), pk2(s[2], s[3])}; }
    bar();
    const unsigned a = L0 + L_SCR + (wk * 136 + h * 17 + (r32 < 16 ? r32 : 16)) * 8;
    const u32x2 c0 = *(LAS u32x2*)a, c1 = *(LAS u32x2*)(a + 272), c2 = *(LAS u32x2*)(a + 544), c3 = *(LAS u32x2*)(a + 816);
    const f32x16 d = mfma(u32x4{c2[0], c2[1], c3[0], c3[1]}, phq1, mfma(u32x4{c0[0], c0[1], c1[0], c1[1]}, phq0, Z16));
    const float rd = __builtin_amdgcn_rcpf(d[8] + 1e-6f); float o[8];
#pragma unroll
    for (int i = 0; i < 8; ++i) o[i] = d[i] * rd;
    __builtin_amdgcn_raw_buffer_store_b128(pk8(o), rs, WS_O + ((mt * 4 + wm) * 32 + nt * 2 + wk) * 1024 + lane * 16, 0, 16);
    publish(ws, 256 + mt * 16 + nt, tag); }
  ST(6);
  load_gu(7, 12);
  const unsigned wb8 = L0 + wq * 2048 + (4 * h + q) * 128 + cb * 32 + p * 8 + (q >> 1) * 64;
  wait_flags(ws, 256 + (Mt >> 1) * 16, 1, 16, tag, wave, lane);
  if (b == 0 && tid == 0) __hip_atomic_store((gu32*)ws, tag, RLX_AGENT);
  ST(7);
  u32x4 x1p;
  {
#pragma unroll
    for (int i = 0; i < 8; ++i) xa[i] = __builtin_amdgcn_raw_buffer_load_b128(rs, WS_O + ((Mt * 2 + wm2) * 32 + wq * 8 + i) * 1024 + lane * 16, 0, 16);
    acc[0] = Z16; acc[1] = Z16;
    const lp wpp[2] = {opq(wb8), opq(wb8 ^ 64)}; u32x4 w[2][2];
    auto ldw = [&](int i) { w[i & 1][0] = fragp(wpp[0], i * 8192, 1024); w[i & 1][1] = fragp(wpp[1], i * 8192, 1024); };
    ST2(19);
    ldw(0); SB0();
#pragma unroll
    for (int i = 0; i < 8; ++i) {
      if (i < 7) ldw(i + 1);
      acc[0] = mfma(w[i & 1][0], xa[i], acc[0]); acc[1] = mfma(w[i & 1][1], xa[i], acc[1]);
      ilv<2, 2, 0>(); SB0();
    }
    bar();
    float r[8]; reduce4(L0, acc, wave, lane, r);
    unsigned xs[4] = {xr[0], xr[1], xr[2], xr[3]}; swap32(xs[0], xs[2]); swap32(xs[1], xs[3]);
    const unsigned ma = L0 + L_MOD + (col2 + 4 * h) * 4; float y[8], s = 0, v = 0;
#pragma unroll
    for (int e = 0; e < 2; ++e) { const f32x4 ga = ldsf4(ma + 4096 + e * 32), ca = ldsf4(ma + 8192 + e * 32);
      x1p[2 * e] = pk2(lo16(xs[2 * e]) + ga[0] * r[4 * e], hi16(xs[2 * e]) + ga[1] * r[4 * e + 1]);
      x1p[2 * e + 1] = pk2(lo16(xs[2 * e + 1]) + ga[2] * r[4 * e + 2], hi16(xs[2 * e + 1]) + ga[3] * r[4 * e + 3]);
#pragma unroll
      for (int i = 0; i < 4; ++i) { r[4 * e + i] = (i & 1) ? hi16(x1p[2 * e + (i >> 1)]) : lo16(x1p[2 * e + (i >> 1)]); y[4 * e + i] = r[4 * e + i] * ca[i]; s += r[4 * e + i]; } }
    __builtin_amdgcn_raw_buffer_store_b128(pk8(y), rs, WS_X1 + ((Mt * 2 + wm2) * 32 + Nt * 4 + wq) * 1024 + lane * 16, 0, 16);
    s = hsum(s) * (1.f / 16.f);
#pragma unroll
    for (int i = 0; i < 8; ++i) v += (r[i] - s) * (r[i] - s);
    v = hsum(v);
    if (h == 0) *(LAS f32x2*)(L0 + L_SCR + wave * 256 + r32 * 8) = f32x2{s, v};
    bar();
    if (tid < 64) { f32x2 a[4];
#pragma unroll
      for (int i = 0; i < 4; ++i) a[i] = *(LAS f32x2*)(L0 + L_SCR + i * 512 + tid * 8);
      const float m = ((a[0][0] + a[1][0]) + (a[2][0] + a[3][0])) * 0.25f; float M = 0;
#pragma unroll
      for (int i = 0; i < 4; ++i) M += a[i][1] + 16.f * (a[i][0] - m) * (a[i][0] - m);
      __builtin_amdgcn_raw_buffer_store_b64(u32x2{__float_as_uint(m), __float_as_uint(M)}, rs, WS_ST + (((Mt * 2 + h) * 4 + (Nt >> 1)) * 32 + r32) * 16 + (Nt & 1) * 8, 0, 16); }
    publish(ws, 512 + Mt * 8 + Nt, tag); }
  ST(8);
  load_gu(0, 2); load_wd(0, 6);
  const unsigned wb3 = L0 + 49152 + (4 * h + q) * 192 + cb * 32 + p * 8;
  asm volatile("s_waitcnt vmcnt(6)" ::: "memory"); bar();
  if (wave >= 2) { const int T = wq - 1; f32x16 d = Z16;
    const lp wpt = opq(wb3 + T * 64 + wm2 * 49152), mp = opq(L0 + L_MOD + ((r32 == 1 ? 1536 : 2048) + wm2 * 256 + 4 * h) * 4);
#pragma unroll
    for (int s = 0; s < 16; ++s) { const f32x4 m0 = ldp4(mp, s * 64), m1 = ldp4(mp, s * 64 + 32);
      d = mfma(fragp(wpt, s * 3072, 1536), u32x4{pk2(m0[0], m0[1]), pk2(m0[2], m0[3]), pk2(m1[0], m1[1]), pk2(m1[2], m1[3])}, d); }
    if (r32 < 2) {
#pragma unroll
      for (int c = 0; c < 4; ++c) stsf4(L0 + L_SCR + ((wave - 2) * 4 + r32 * 2 + h) * 64 + c * 16, f32x4{d[4 * c], d[4 * c + 1], d[4 * c + 2], d[4 * c + 3]}); } }
  wait_flags(ws, 512 + m3 * 32, 1, 32, tag, wave, lane);
  ST(9);
  { const int tt = m3 * 8 + wave;
#pragma unroll
    for (int s = 0; s < 32; ++s) xa[s] = __builtin_amdgcn_raw_buffer_load_b128(rs, WS_X1 + (tt * 32 + s) * 1024 + lane * 16, 0, 16);
    const f32x4 s0 = __builtin_bit_cast(f32x4, __builtin_amdgcn_raw_buffer_load_b128(rs, WS_ST + ((tt * 4 + 2 * h) * 32 + r32) * 16, 0, 16));
    const f32x4 s1 = __builtin_bit_cast(f32x4, __builtin_amdgcn_raw_buffer_load_b128(rs, WS_ST + ((tt * 4 + 2 * h + 1) * 32 + r32) * 16, 0, 16));
    { const unsigned ua = L0 + L_SCR + (lane >> 4) * 512 + (lane & 15) * 16;
      if (lane < 48) stsf4(L0 + L_SCR + 2048 + lane * 16, ldsf4(ua) + ldsf4(ua + 256)); }
    acc[0] = Z16; acc[1] = Z16; acc[2] = Z16;
    const lp wpp[2] = {opq(wb3), opq(wb3 + 49152)}; u32x4 w[2][3];
    auto ldw = [&](int s) {
#pragma unroll
      for (int c = 0; c < 3; ++c) w[s & 1][c] = fragp(wpp[s >> 4], (s & 15) * 3072 + c * 64, 1536); };
    ST2(21);
    ldw(0); SB0();
#pragma unroll
    for (int s = 0; s < 32; ++s) {
      if (s < 31) ldw(s + 1);
#pragma unroll
      for (int c = 0; c < 3; ++c) acc[c] = mfma(w[s & 1][c], xa[s], acc[c]);
      ilv<3, 2, 0>(); SB0();
    }
    ST(10);
    const float mean = hsum((s0[0] + s0[2]) + (s1[0] + s1[2])) * 0.125f;
    const float d0 = s0[0] - mean, d1 = s0[2] - mean, d2 = s1[0] - mean, d3 = s1[2] - mean;
    const float var = hsum((s0[1] + s0[3]) + (s1[1] + s1[3]) + 64.f * (d0 * d0 + d1 * d1 + d2 * d2 + d3 * d3)) * (1.f / 512.f);
    const float rstd = rsqrtf(var + 1e-5f), nmr = -mean * rstd;
#pragma unroll
    for (int T = 0; T < 3; ++T) { float r[16];
#pragma unroll
      for (int c = 0; c < 4; ++c) { const f32x4 u = ldsf4(L0 + L_SCR + 2048 + T * 256 + h * 64 + c * 16), v = ldsf4(L0 + L_SCR + 2048 + T * 256 + 128 + h * 64 + c * 16);
#pragma unroll
        for (int i = 0; i < 4; ++i) r[4 * c + i] = acc[T][4 * c + i] * rstd + (u[i] * nmr + v[i]); }
#pragma unroll
      for (int i = 0; i < 8; ++i) r[i] = r[8 + i] * __builtin_amdgcn_rcpf(1.f + __builtin_amdgcn_exp2f(-1.4426950408889634f * r[i]));
      if (ul + T >= u0 && ul + T < u0 + nu) __builtin_amdgcn_raw_buffer_store_b128(pk8(r), rs, WS_H + (tt * 86 + ul + T) * 1024 + lane * 16, 0, 16);
    }
    publish(ws, 768 + m3 * 32 + nf, tag); }
  ST(11);
  load_wd(6, 18);
  wait_flags(ws, 768 + (Mt >> 2) * 32, 1, 32, tag, wave, lane);
  ST(12);
  { const unsigned hb = WS_H + ((Mt * 2 + wm2) * 86 + ks4) * 1024 + lane * 16;
#pragma unroll
    for (int i = 0; i < 21; ++i) xa[i] = __builtin_amdgcn_raw_buffer_load_b128(rs, hb + i * 1024, 0, 16);
    xa[21] = __builtin_amdgcn_raw_buffer_load_b128(rs, hb + (wq < 2 ? 21 : 20) * 1024, 0, 16);
    acc[0] = Z16; acc[1] = Z16;
    lp wpp[3][2]; u32x4 w[2][2];
#pragma unroll
    for (int i = 0; i < 6; ++i) wpp[i >> 1][i & 1] = opq((wb8 ^ ((i & 1) * 64)) + (i >> 1) * 49152);
    auto ldw = [&](int i) { const int sl = i < 18 ? i : i - 18; w[i & 1][0] = fragp(wpp[sl / 6][0], (sl % 6) * 8192, 1024); w[i & 1][1] = fragp(wpp[sl / 6][1], (sl % 6) * 8192, 1024); };
    ST2(23);
    ldw(0); SB0();
#pragma unroll
    for (int i = 0; i < 22; ++i) {
      if (i == 3) { asm volatile("s_waitcnt vmcnt(22)" ::: "memory"); bar(); load_wd(18, 22); }
      if (i == 17) { wait_vm0(); bar(); }
      if (i < 21) ldw(i + 1);
      if (i == 21 && wq >= 2) xa[21] = u32x4{0, 0, 0, 0};
      acc[0] = mfma(w[i & 1][0], xa[i], acc[0]); acc[1] = mfma(w[i & 1][1], xa[i], acc[1]);
      ilv<2, 2, 0>(); SB0();
    }
    ST(13);
    float r[8]; reduce4(L0 + 49152, acc, wave, lane, r);
    unsigned o[4];
#pragma unroll
    for (int e = 0; e < 2; ++e) { const f32x4 gb = ldsf4(L0 + L_MOD + (2560 + col2 + 4 * h) * 4 + e * 32);
      o[2 * e] = pk2(lo16(x1p[2 * e]) + gb[0] * r[4 * e], hi16(x1p[2 * e]) + gb[1] * r[4 * e + 1]);
      o[2 * e + 1] = pk2(lo16(x1p[2 * e + 1]) + gb[2] * r[4 * e + 2], hi16(x1p[2 * e + 1]) + gb[3] * r[4 * e + 3]); }
    swap32(o[0], o[2]); swap32(o[1], o[3]);
    *(u32x4*)(out + tok2 * 512 + col2 + 8 * h) = u32x4{o[0], o[1], o[2], o[3]};
  }
  ST(14);
}

extern "C" void kernel_launch(void* const* d_in, const int* in_sizes, int n_in, void* d_out, int out_size, void* d_ws, size_t ws_size,
                              hipStream_t stream) {
  typedef const unsigned short* P;
  adaln_block<<<256, 512, 0, stream>>>((P)d_in[0], (P)d_in[1], (P)d_in[2], (P)d_in[3], (P)d_in[5], (P)d_in[6], (P)d_in[7], (P)d_in[8], (P)d_in[9],
                                       (unsigned short*)d_out, (char*)d_ws);
}
```

```cpp
#include <hip/hip_runtime.h>
#include <hip/hip_bf16.h>
#include <type_traits>

typedef __attribute__((ext_vector_type(8))) __bf16 bf16x8;
typedef __attribute__((ext_vector_type(2))) __bf16 bf16x2;
typedef __attribute__((ext_vector_type(4))) short s16x4;
typedef __attribute__((ext_vector_type(4))) float f32x4;
typedef __attribute__((ext_vector_type(2))) float f32x2;
typedef __attribute__((ext_vector_type(16))) float f32x16;
typedef __attribute__((ext_vector_type(4))) unsigned u32x4;
typedef __attribute__((ext_vector_type(2))) unsigned u32x2;
typedef __attribute__((address_space(1))) unsigned gu32;
typedef __attribute__((address_space(1))) unsigned long long gu64;
#define LAS __attribute__((address_space(3)))
#define RLX_AGENT __ATOMIC_RELAXED, __HIP_MEMORY_SCOPE_AGENT
#define DEV __device__ __forceinline__

constexpr unsigned WS_FLAG = 4096;
constexpr unsigned WS_MOD = WS_FLAG + 4 * 256 * 128;
constexpr unsigned WS_KV = WS_MOD + 16384;
constexpr unsigned WS_ST = WS_KV + 256 * 4352;
constexpr unsigned WS_O = WS_ST + 2048 * 128;
constexpr unsigned WS_X1 = WS_O + 2097152;
constexpr unsigned WS_H = WS_X1 + 2097152;
constexpr unsigned WS_END = WS_H + 128 * 43 * 1024;
constexpr unsigned L_MOD = 147456, L_SCR = 159744;
constexpr unsigned SPIN_CAP = 1u << 19;

DEV unsigned pk2(float a, float b) { f32x2 v = {a, b}; return __builtin_bit_cast(unsigned, __builtin_convertvector(v, bf16x2)); }
DEV float lo16(unsigned u) { return __uint_as_float(u << 16); }
DEV float hi16(unsigned u) { return __uint_as_float(u & 0xffff0000u); }
DEV float rbf(float a) { return lo16(pk2(a, a)); }
DEV f32x16 mfma(u32x4 a, u32x4 b, f32x16 c) {
  return __builtin_amdgcn_mfma_f32_32x32x16_bf16(__builtin_bit_cast(bf16x8, a), __builtin_bit_cast(bf16x8, b), c, 0, 0, 0); }
DEV u32x4 pk8(const float* v) { return u32x4{pk2(v[0], v[1]), pk2(v[2], v[3]), pk2(v[4], v[5]), pk2(v[6], v[7])}; }
DEV void swap32(unsigned& a, unsigned& b) { auto s = __builtin_amdgcn_permlane32_swap(a, b, false, false); a = s[0]; b = s[1]; }
DEV float hsum(float x) { unsigned a = __float_as_uint(x), b = a; swap32(a, b); return __uint_as_float(a) + __uint_as_float(b); }
DEV float hmax(float x) { unsigned a = __float_as_uint(x), b = a; swap32(a, b); return fmaxf(__uint_as_float(a), __uint_as_float(b)); }
#define QSEL(o0, o1, mv0, keep0, mv1, keep1, MASK, QP) asm("s_mov_b32 vcc_lo, " MASK "\n\ts_mov_b32 vcc_hi, " MASK "\n\ts_nop 1\n\t" \
  "v_cndmask_b32_dpp %0, %8, %16, vcc " QP "\n\tv_cndmask_b32_dpp %1, %9, %17, vcc " QP "\n\tv_cndmask_b32_dpp %2, %10, %18, vcc " QP "\n\tv_cndmask_b32_dpp %3, %11, %19, vcc " QP "\n\t" \
  "v_cndmask_b32_dpp %4, %12, %20, vcc " QP "\n\tv_cndmask_b32_dpp %5, %13, %21, vcc " QP "\n\tv_cndmask_b32_dpp %6, %14, %22, vcc " QP "\n\tv_cndmask_b32_dpp %7, %15, %23, vcc " QP \
  : "=&v"(o0[0]), "=&v"(o0[1]), "=&v"(o0[2]), "=&v"(o0[3]), "=&v"(o1[0]), "=&v"(o1[1]), "=&v"(o1[2]), "=&v"(o1[3]) \
  : "v"(mv0[0]), "v"(mv0[1]), "v"(mv0[2]), "v"(mv0[3]), "v"(mv1[0]), "v"(mv1[1]), "v"(mv1[2]), "v"(mv1[3]), \
    "v"(keep0[0]), "v"(keep0[1]), "v"(keep0[2]), "v"(keep0[3]), "v"(keep1[0]), "v"(keep1[1]), "v"(keep1[2]), "v"(keep1[3]) : "vcc")
#define QX1 "quad_perm:[1,0,3,2] row_mask:0xf bank_mask:0xf"
#define QX2 "quad_perm:[2,3,0,1] row_mask:0xf bank_mask:0xf"
#define XRED(name, OP) DEV float name(float x) { \
  auto s = __builtin_amdgcn_permlane16_swap(__float_as_uint(x), __float_as_uint(x), false, false); \
  x = OP(__uint_as_float(s[0]), __uint_as_float(s[1])); \
  auto t = __builtin_amdgcn_permlane32_swap(__float_as_uint(x), __float_as_uint(x), false, false); \
  return OP(__uint_as_float(t[0]), __uint_as_float(t[1])); }
DEV float addf(float a, float b) { return a + b; }
XRED(xsum, addf)
template <int N> DEV float ror(float x) { return __int_as_float(__builtin_amdgcn_update_dpp(0, __float_as_int(x), 0x120 + N, 0xf, 0xf, false)); }

DEV void glds(const void* g, unsigned lds) {
  unsigned keep;
  asm volatile("s_mov_b32 %0, m0\n\ts_mov_b32 m0, %2\n\ts_nop 0\n\tglobal_load_lds_dwordx4 %1, off\n\ts_mov_b32 m0, %0"
               : "=&s"(keep) : "v"(g), "s"(lds) : "memory");
}
DEV void wait_vm0() { asm volatile("s_waitcnt vmcnt(0)" ::: "memory"); }
DEV void bar() { asm volatile("s_waitcnt lgkmcnt(0)\n\ts_barrier" ::: "memory"); }
typedef LAS char* lp;
DEV lp opq(unsigned a) { asm volatile("" : "+v"(a)); return (lp)a; }
DEV u32x4 fragp(lp a, int o, int d) {
  u32x2 x = __builtin_bit_cast(u32x2, __builtin_amdgcn_ds_read_tr16_b64_v4i16((LAS s16x4*)(a + o)));
  u32x2 y = __builtin_bit_cast(u32x2, __builtin_amdgcn_ds_read_tr16_b64_v4i16((LAS s16x4*)(a + o + d)));
  return u32x4{x[0], x[1], y[0], y[1]}; }
DEV f32x4 ldp4(lp a, int o) { return *(LAS f32x4*)(a + o); }
template <int I, int N, class F> DEV void sfor(F f) { if constexpr (I < N) { f(std::integral_constant<int, I>{}); sfor<I + 1, N>(f); } }
#define SB0() __builtin_amdgcn_sched_barrier(0)
template <int NM, int ND, int NV> DEV void ilv() {
#pragma unroll
  for (int i = 0; i < NM; ++i) { __builtin_amdgcn_sched_group_barrier(0x008, 1, 0); __builtin_amdgcn_sched_group_barrier(0x100, ND, 0);
    if (NV) __builtin_amdgcn_sched_group_barrier(0x002, NV, 0); }
}
DEV f32x4 ldsf4(unsigned a) { return *(LAS f32x4*)a; }
DEV void stsf4(unsigned a, f32x4 v) { *(LAS f32x4*)a = v; }

DEV void wait_flags(char* ws, unsigned first, unsigned stride, unsigned n, unsigned tag, int wave, int lane) {
  if (wave == 0) {
    gu32* p = (gu32*)(ws + WS_FLAG) + (first + (lane & (n - 1)) * stride) * 32;
    for (unsigned s = 0; s < SPIN_CAP; ++s) {
      if (__all(__hip_atomic_load(p, RLX_AGENT) == tag)) break;
      __builtin_amdgcn_s_sleep(1);
    }
  }
  bar();
}
DEV void publish(char* ws, unsigned idx, unsigned tag) {
  wait_vm0(); bar();
  if (threadIdx.x == 0) __hip_atomic_store((gu32*)(ws + WS_FLAG) + idx * 32, tag, RLX_AGENT);
}
DEV unsigned poll_granule(char* ws, unsigned idx, unsigned tag) {
  gu64* p = (gu64*)(ws + WS_MOD) + idx; unsigned long long v = 0;
  for (unsigned s = 0; s < SPIN_CAP; ++s) {
    v = __hip_atomic_load(p, RLX_AGENT);
    if (__all((unsigned)(v >> 32) == tag)) break;
    __builtin_amdgcn_s_sleep(1);
  }
  return (unsigned)v;
}
#define ST(i)
#define ST2(i)

DEV void hedgehog(u32x4 wp, const float* v, u32x4& o0, u32x4& o1) {
  const float L = 1.4426950408889634f; f32x16 Z = {0}; f32x16 pp = mfma(wp, pk8(v), Z); float e[16];
#pragma unroll
  for (int s = 0; s < 2; ++s) {
    float m = fmaxf(fmaxf(fmaxf(pp[8 * s], pp[8 * s + 1]), fmaxf(pp[8 * s + 2], pp[8 * s + 3])), fmaxf(fmaxf(pp[8 * s + 4], pp[8 * s + 5]), fmaxf(pp[8 * s + 6], pp[8 * s + 7])));
    m = -hmax(m) * L; float t = 0;
#pragma unroll
    for (int i = 0; i < 8; ++i) { e[8 * s + i] = __builtin_amdgcn_exp2f(pp[8 * s + i] * L + m); t += e[8 * s + i]; }
    t = __builtin_amdgcn_rcpf(hsum(t));
#pragma unroll
    for (int i = 0; i < 8; ++i) e[8 * s + i] *= t;
  }
  o0 = pk8(e); o1 = pk8(e + 8);
}
DEV void reduce4(unsigned base, const f32x16 (&acc)[3], int wave, int lane, float* r) {
  const int wm = wave & 1, wq = wave >> 1;
#pragma unroll
  for (int d = 0; d < 4; ++d) {
    const f32x16 v = acc[d >> 1]; const int o = (d & 1) * 8;
    if (d != wq) { const unsigned a = base + ((d * 2 + wm) * 3 + ((wq - d - 1) & 3)) * 2048 + lane * 16;
      stsf4(a, f32x4{v[o], v[o + 1], v[o + 2], v[o + 3]}); stsf4(a + 1024, f32x4{v[o + 4], v[o + 5], v[o + 6], v[o + 7]}); }
    else {
#pragma unroll
      for (int i = 0; i < 8; ++i) r[i] = v[o + i]; }
  }
  bar();
#pragma unroll
  for (int s = 0; s < 6; ++s) { const f32x4 v = ldsf4(base + wave * 6144 + s * 1024 + lane * 16);
#pragma unroll
    for (int i = 0; i < 4; ++i) r[(s & 1) * 4 + i] += v[i]; }
}

__global__ void __launch_bounds__(512, 2)
adaln_block(const unsigned short* __restrict__ x, const unsigned short* __restrict__ wqkv, const unsigned short* __restrict__ wphi,
            const unsigned short* __restrict__ wo, const unsigned short* __restrict__ wg, const unsigned short* __restrict__ wu,
            const unsigned short* __restrict__ wd, const unsigned short* __restrict__ cin, const unsigned short* __restrict__ wmod,
            unsigned short* __restrict__ out, char* ws) {
  __shared__ __attribute__((aligned(16))) char smem[163840];
  const unsigned L0 = (unsigned)(unsigned long long)(LAS char*)smem;
  const int tid = threadIdx.x, lane = tid & 63, wave = __builtin_amdgcn_readfirstlane(tid >> 6);
  const int h = lane >> 5, r32 = lane & 31, cb = (lane >> 4) & 1, q = (lane & 15) >> 2, p = lane & 3;
  const int b = blockIdx.x, xc = b & 7, j = b >> 3;
  const int mt = (xc >> 1) * 4 + (j & 3), nt = (xc & 1) * 8 + (j >> 2);
  const int Mt = (xc >> 1) * 8 + (j & 7), Nt = (xc & 1) * 4 + (j >> 3);
  const int m3 = (xc >> 1) * 2 + (j & 1), nf = (xc & 1) * 16 + (j >> 1);
  const int wm = wave & 3, wk = wave >> 2, wm2 = wave & 1, wq = wave >> 1;
  const __amdgpu_buffer_rsrc_t rs = __builtin_amdgcn_make_buffer_rsrc(ws, 0, WS_END, 0x00020000);
  const unsigned wbase = L0 + wave * 1024;
  const f32x16 Z16 = {0};
  ST(0);
  const unsigned tag = __builtin_amdgcn_readfirstlane(__hip_atomic_load((gu32*)ws, RLX_AGENT)) + 1u;

  u32x4 wv1[4], wv2[4]; unsigned short cv1[4], cv2[4];
  auto gemv_ld = [&](u32x4 (&wv)[4], unsigned short (&cv)[4]) {
#pragma unroll
    for (int i = 0; i < 4; ++i) { int row = (tid >> 2) + 128 * i; wv[i] = *(const u32x4*)(wmod + row * 3072 + b * 32 + (tid & 3) * 8); cv[i] = cin[row]; } };
  auto gemv_fin = [&](u32x4 (&wv)[4], unsigned short (&cv)[4]) {
    f32x2 a[4] = {{0, 0}, {0, 0}, {0, 0}, {0, 0}};
#pragma unroll
    for (int i = 0; i < 4; ++i) {
      float c = lo16(cv[i]); c = rbf(c * __builtin_amdgcn_rcpf(1.f + __expf(-c)));
#pragma unroll
      for (int e = 0; e < 4; ++e) a[e] += c * f32x2{lo16(wv[i][e]), hi16(wv[i][e])};
    }
#pragma unroll
    for (int e = 0; e < 8; ++e) { float v = a[e >> 1][e & 1]; v += ror<4>(v); v += ror<8>(v); a[e >> 1][e & 1] = v; }
    if ((lane & 12) == 0) { const unsigned d = L0 + L_MOD + (tid >> 4) * 128 + p * 32;
      stsf4(d, f32x4{a[0][0], a[0][1], a[1][0], a[1][1]}); stsf4(d + 16, f32x4{a[2][0], a[2][1], a[3][0], a[3][1]}); }
    bar();
    if (wave == 0) { f32x2 s = {0, 0};
#pragma unroll
      for (int w = 0; w < 8; ++w) s += *(LAS f32x2*)(L0 + L_MOD + w * 512 + lane * 8);
      s[0] = xsum(s[0]); s[1] = xsum(s[1]);
      if (lane < 16) __hip_atomic_store((gu64*)(ws + WS_MOD) + b * 16 + lane, ((unsigned long long)tag << 32) | pk2(s[0], s[1]), RLX_AGENT); } };
  if (b < 32) gemv_ld(wv1, cv1);
  u32x4 xa[32];
  { const unsigned short* xp = x + (mt * 128 + wm * 32 + (r32 & 28)) * 512 + wk * 256 + h * 32 + p * 8;
#pragma unroll
    for (int i = 0; i < 16; ++i) xa[i] = *(const u32x4*)(xp + (i & 3) * 512 + (i >> 2) * 64); }
  u32x4 wp;
  { const unsigned short* pp = wphi + (nt * 2 + wk) * 256 + 4 * h * 16 + (lane & 15); const unsigned sg = cb ? 0x80008000u : 0u;
#pragma unroll
    for (int e = 0; e < 4; ++e) { const int d = (e >> 1) * 8 + (e & 1) * 2; wp[e] = ((unsigned)pp[d * 16] | ((unsigned)pp[d * 16 + 16] << 16)) ^ sg; } }
  const int u0 = nf < 22 ? 3 * nf : 2 * nf + 22, nu = nf < 22 ? 3 : 2, ul = u0 > 83 ? 83 : u0;
  const unsigned short *gsrc[3], *qsrc[3];
#pragma unroll
  for (int i = 0; i < 3; ++i) {
    const int c0 = i * 512 + tid, row = c0 / 12, cc = c0 - row * 12;
    qsrc[i] = wqkv + row * 1536 + (cc >> 2) * 512 + nt * 32 + (cc & 3) * 8;
  }
  auto load_gu = [&](int n0, int n1) {
#pragma unroll
    for (int n = n0; n < n1; ++n) glds(gsrc[n % 3] + (n / 3) * 128 * 1376, wbase + 49152 + n * 8192); };
  const int off8 = (wm2 * 8 + (lane >> 3)) * 512 + Nt * 64 + ((((lane >> 2) ^ (lane >> 4)) & 1) * 32) + p * 8;
  const int ks4 = wq * 22 - (wq == 3);
  auto load_wd = [&](int i0, int i1) {
#pragma unroll
    for (int i = i0; i < i1; ++i) glds(wd + off8 + (i == 21 && wq == 3 ? 85 : ks4 + i) * 8192, wbase + (i < 18 ? i : i - 18) * 8192); };

  auto load_wo = [&](int i0, int i1) {
#pragma unroll
    for (int i = i0; i < i1; ++i) glds(wo + off8 + (wq * 8 + i) * 8192, wbase + (i < 6 ? 98304 : 0) + i * 8192); };
  if (b < 32) gemv_fin(wv1, cv1);
  asm volatile("" : "+v"(xa[0]), "+v"(xa[1]), "+v"(xa[2]), "+v"(xa[3]), "+v"(xa[4]), "+v"(xa[5]), "+v"(xa[6]), "+v"(xa[7]), "+v"(xa[8]), "+v"(xa[9]),
               "+v"(xa[10]), "+v"(xa[11]), "+v"(xa[12]), "+v"(xa[13]), "+v"(xa[14]), "+v"(xa[15]), "+v"(wp));
  ST(15);
  f32x2 t[64];
  { f32x2 sv = {0, 0}, vv = {0, 0};
#pragma unroll
    for (int s = 0; s < 4; ++s) { unsigned n[4][4], m[4][4]; const u32x4 r0 = xa[4 * s], r1 = xa[4 * s + 1], r2 = xa[4 * s + 2], r3 = xa[4 * s + 3];
#pragma unroll
      for (int i = 0; i < 3; ++i) glds(qsrc[i] + s * 128 * 1536, wbase + (s * 3 + i) * 8192);
      SB0();
      QSEL(n[1], n[3], r0, r1, r2, r3, "0xaaaaaaaa", QX1); QSEL(n[0], n[2], r1, r0, r3, r2, "0x55555555", QX1);
      QSEL(m[2], m[3], n[0], n[2], n[1], n[3], "0xcccccccc", QX2); QSEL(m[0], m[1], n[2], n[0], n[3], n[1], "0x33333333", QX2);
#pragma unroll
      for (int i = 0; i < 16; ++i) { t[16 * s + i] = f32x2{lo16(m[i >> 2][i & 3]), hi16(m[i >> 2][i & 3])}; sv += t[16 * s + i]; }
      SB0(); }
    ST(16);
    const float mh = hsum(sv[0] + sv[1]) * (1.f / 256.f);
#pragma unroll
    for (int i = 0; i < 64; ++i) { t[i] -= mh; vv += t[i] * t[i]; }
    const float vh = hsum(vv[0] + vv[1]);
    if (h == 0) *(LAS f32x2*)(L0 + L_SCR + 1024 + wave * 256 + r32 * 8) = f32x2{mh, vh};
    bar();
    const f32x2 o = *(LAS f32x2*)(L0 + L_SCR + 1024 + (wave ^ 4) * 256 + r32 * 8);
    const float dm = mh - o[0], rstd = rsqrtf((vh + o[1] + 128.f * dm * dm) * (1.f / 512.f) + 1e-5f), dl = 0.5f * dm * rstd;
#pragma unroll
    for (int i = 0; i < 64; ++i) t[i] = t[i] * rstd + dl; }
  ST(1);
  float ms[8], mc[8];
  { const unsigned go = WS_MOD + (wk * 256 + q * 64 + h * 32 + p * 8) * 4; u32x4 g[4];
    for (unsigned s = 0; s < SPIN_CAP; ++s) { bool ok = true;
#pragma unroll
      for (int i = 0; i < 4; ++i) { g[i] = __builtin_amdgcn_raw_buffer_load_b128(rs, go + (i >> 1) * 2048 + (i & 1) * 16, 0, 16); ok = ok && g[i][1] == tag && g[i][3] == tag; }
      if (__all(ok)) break;
      asm volatile("s_sleep 1" ::: "memory");
    }
#pragma unroll
    for (int i = 0; i < 4; ++i) { const unsigned u = g[i >> 1][(i & 1) * 2], v = g[2 + (i >> 1)][(i & 1) * 2], c = pk2(1.f + lo16(v), 1.f + hi16(v));
      ms[2 * i] = lo16(u); ms[2 * i + 1] = hi16(u); mc[2 * i] = lo16(c); mc[2 * i + 1] = hi16(c); } }
  wait_vm0(); bar();
  ST(2);
  load_wo(0, 6);
  f32x16 acc[3] = {Z16, Z16, Z16};
  { const lp wb = opq(L0 + (wk * 256 + h * 32 + q) * 192 + cb * 32 + p * 8);
    u32x4 w[2][3], hh[2];
    auto ldw = [&](int st, int c) { w[st & 1][c] = fragp(wb, ((st >> 2) * 64 + (st & 3) * 8) * 192 + c * 64, 768); };
    auto mkh = [&](auto S, int e) {
      constexpr int st = S; float v0 = t[st * 4 + e][0], v1 = t[st * 4 + e][1];
      asm("v_mul_f32_dpp %0, %2, %0 row_newbcast:%6 row_mask:0xf bank_mask:0xf\n\tv_mul_f32_dpp %1, %3, %1 row_newbcast:%6 row_mask:0xf bank_mask:0xf\n\t"
          "v_add_f32_dpp %0, %4, %0 row_newbcast:%6 row_mask:0xf bank_mask:0xf\n\tv_add_f32_dpp %1, %5, %1 row_newbcast:%6 row_mask:0xf bank_mask:0xf"
          : "+v"(v0), "+v"(v1) : "v"(mc[2 * e]), "v"(mc[2 * e + 1]), "v"(ms[2 * e]), "v"(ms[2 * e + 1]), "i"(st));
      hh[st & 1][e] = pk2(v0, v1); };
#pragma unroll
    for (int e = 0; e < 8; ++e) asm volatile("" : "+v"(ms[e]), "+v"(mc[e]));
    asm volatile("s_nop 1"); SB0();
    ldw(0, 0); ldw(0, 1); ldw(0, 2);
#pragma unroll
    for (int e = 0; e < 4; ++e) mkh(std::integral_constant<int, 0>{}, e);
    SB0();
    sfor<0, 16>([&](auto S) { constexpr int st = S; std::integral_constant<int, (st + 1) & 15> N;
      acc[0] = mfma(w[st & 1][0], hh[st & 1], acc[0]); SB0();
      if (st < 15) { ldw(st + 1, 0); mkh(N, 0); mkh(N, 1); }
      SB0(); acc[1] = mfma(w[st & 1][1], hh[st & 1], acc[1]); SB0();
      if (st < 15) { ldw(st + 1, 1); mkh(N, 2); }
      SB0(); acc[2] = mfma(w[st & 1][2], hh[st & 1], acc[2]); SB0();
      if (st < 15) { ldw(st + 1, 2); mkh(N, 3); }
      SB0(); }); }
  ST(3);
  u32x4 phq0, phq1;
  { float v[3][8];
    bar(); load_wo(6, 8);
    auto send = [&](int o) {
#pragma unroll
      for (int s = 0; s < 6; ++s) { const f32x16 a = acc[s >> 1]; const int i = o + (s & 1) * 4; stsf4(L0 + wave * 6144 + s * 1024 + lane * 16, f32x4{a[i], a[i + 1], a[i + 2], a[i + 3]}); } };
    auto recv = [&](int o) {
#pragma unroll
      for (int s = 0; s < 6; ++s) { const f32x4 r = ldsf4(L0 + (wave ^ 4) * 6144 + s * 1024 + lane * 16);
#pragma unroll
        for (int i = 0; i < 4; ++i) v[s >> 1][(s & 1) * 4 + i] = acc[s >> 1][o + (s & 1) * 4 + i] + r[i]; } };
    if (wk) { send(0); bar(); recv(8); } else { send(8); bar(); recv(0); }
    ST(17);
    u32x4 k0, k1; hedgehog(wp, v[1], k0, k1);
    const u32x4 vb = pk8(v[2]);
    const unsigned img = L0 + 65536 + wave * 4096, wr = img + r32 * 104 + h * 8;
    *(LAS u32x2*)(wr) = u32x2{k0[0], k0[1]}; *(LAS u32x2*)(wr + 16) = u32x2{k0[2], k0[3]}; *(LAS u32x2*)(wr + 32) = u32x2{k1[0], k1[1]}; *(LAS u32x2*)(wr + 48) = u32x2{k1[2], k1[3]};
    *(LAS u32x2*)(wr + 64) = u32x2{vb[0], vb[1]}; *(LAS u32x2*)(wr + 80) = u32x2{vb[2], vb[3]};
    asm volatile("" ::: "memory");
    f32x16 kv = Z16; const lp ra = opq(img + (8 * h + q) * 104 + cb * 32 + p * 8), rb = opq(img + (8 * h + q) * 104 + 64 + p * 8);
#pragma unroll
    for (int s = 0; s < 2; ++s) { u32x4 bb = fragp(rb, s * 1664, 416);
#pragma unroll
      for (int e = 0; e < 4; ++e) bb[e] = cb ? 0x3f803f80u : bb[e];
      kv = mfma(fragp(ra, s * 1664, 416), bb, kv); }
    asm volatile("" ::: "memory");
    ST(18);
    if (r32 <= 16) {
#pragma unroll
      for (int c = 0; c < 4; ++c) stsf4(img + (c * 34 + h * 17 + r32) * 16, f32x4{kv[4 * c], kv[4 * c + 1], kv[4 * c + 2], kv[4 * c + 3]}); }
    bar();
    if (tid < 272) { const unsigned a = L0 + 65536 + (tid < 136 ? tid * 16 : 16384 + (tid - 136) * 16);
      const f32x4 s = (ldsf4(a) + ldsf4(a + 4096)) + (ldsf4(a + 8192) + ldsf4(a + 12288));
      __builtin_amdgcn_raw_buffer_store_b128(__builtin_bit_cast(u32x4, s), rs, WS_KV + (nt * 16 + mt) * 4352 + tid * 16, 0, 16); }
    ST(20);
    publish(ws, mt * 16 + nt, tag);
    ST(4);
    if (b >= 32 && b < 96) gemv_ld(wv2, cv2);
    int tid2 = tid; asm volatile("" : "+v"(tid2));
#pragma unroll
    for (int i = 0; i < 3; ++i) { const int c0 = i * 512 + tid2, row = c0 / 12, cc = c0 - row * 12;
      gsrc[i] = ((cc & 2) ? wu : wg) + row * 1376 + (ul + (cc >> 2)) * 16 + (cc & 1) * 8; }
    load_gu(2, 6);
    hedgehog(wp, v[0], phq0, phq1); }
  const int tok2 = Mt * 64 + wm2 * 32 + r32, col2 = Nt * 64 + wq * 16;
  const u32x4 xr = *(const u32x4*)(x + tok2 * 512 + col2 + 8 * h);
  const unsigned wb8 = L0 + wq * 2048 + (4 * h + q) * 128 + cb * 32 + p * 8 + (q >> 1) * 64;
  u32x4 wf[8][2];
  { const lp wa[4] = {opq(wb8 + 98304), opq((wb8 ^ 64) + 98304), opq(wb8), opq(wb8 ^ 64)};
#pragma unroll
    for (int i = 0; i < 16; ++i) wf[i >> 1][i & 1] = fragp(wa[(i & 1) + (i < 12 ? 0 : 2)], (i >> 1) * 8192, 1024); }
  if (b >= 32 && b < 96) gemv_fin(wv2, cv2);
  wait_flags(ws, nt, 16, 16, tag, wave, lane);
  ST(5);
  { if (tid < 272) { u32x4 v[16];
#pragma unroll
      for (int i = 0; i < 16; ++i) v[i] = __builtin_amdgcn_raw_buffer_load_b128(rs, WS_KV + (nt * 16 + i) * 4352 + tid * 16, 0, 16);
      f32x4 s = __builtin_bit_cast(f32x4, v[0]);
#pragma unroll
      for (int i = 1; i < 16; ++i) s += __builtin_bit_cast(f32x4, v[i]);
      *(LAS u32x2*)(L0 + L_SCR + tid * 8) = u32x2{pk2(s[0], s[1]), pk2(s[2], s[3])}; }
    bar();
    ST(22);
    const unsigned a = L0 + L_SCR + (wk * 136 + h * 17 + (r32 < 16 ? r32 : 16)) * 8;
    const u32x2 c0 = *(LAS u32x2*)a, c1 = *(LAS u32x2*)(a + 272), c2 = *(LAS u32x2*)(a + 544), c3 = *(LAS u32x2*)(a + 816);
    const f32x16 d = mfma(u32x4{c2[0], c2[1], c3[0], c3[1]}, phq1, mfma(u32x4{c0[0], c0[1], c1[0], c1[1]}, phq0, Z16));
    const float rd = __builtin_amdgcn_rcpf(d[8] + 1e-6f); float o[8];
#pragma unroll
    for (int i = 0; i < 8; ++i) o[i] = d[i] * rd;
    __builtin_amdgcn_raw_buffer_store_b128(pk8(o), rs, WS_O + ((mt * 4 + wm) * 32 + nt * 2 + wk) * 1024 + lane * 16, 0, 16);
    ST(24);
    publish(ws, 256 + mt * 16 + nt, tag); }
  ST(6);
  load_gu(0, 2); load_gu(6, 9);
#pragma unroll
  for (int i = 1; i < 3; ++i) { unsigned m = poll_granule(ws, i * 512 + tid, tag);
    if (i == 2 && tid < 256) m = pk2(1.f + lo16(m), 1.f + hi16(m));
    *(LAS f32x2*)(L0 + L_MOD + (i * 512 + tid) * 8) = f32x2{lo16(m), hi16(m)};
    if ((i == 1) == (tid >= 256)) *(LAS unsigned*)(L0 + L_MOD + (2 - i) * 1024 + (tid & 248) * 4 + ((tid >> 1) & 1) * 16 + ((tid >> 2) & 1) * 8 + (tid & 1) * 4) = m; }
  wait_flags(ws, 256 + (Mt >> 1) * 16, 1, 16, tag, wave, lane);
  if (b == 0 && tid == 0) __hip_atomic_store((gu32*)ws, tag, RLX_AGENT);
  ST(7);
  u32x4 x1p;
  {
#pragma unroll
    for (int i = 0; i < 8; ++i) xa[i] = __builtin_amdgcn_raw_buffer_load_b128(rs, WS_O + ((Mt * 2 + wm2) * 32 + wq * 8 + i) * 1024 + lane * 16, 0, 16);
    acc[0] = Z16; acc[1] = Z16;
#pragma unroll
    for (int i = 0; i < 8; ++i) { acc[0] = mfma(wf[i][0], xa[i], acc[0]); acc[1] = mfma(wf[i][1], xa[i], acc[1]); SB0(); }
    load_gu(9, 12);
    ST(25);
    float r[8]; reduce4(L0, acc, wave, lane, r);
    unsigned xs[4] = {xr[0], xr[1], xr[2], xr[3]}; swap32(xs[0], xs[2]); swap32(xs[1], xs[3]);
    const unsigned ma = L0 + L_MOD + (col2 + 4 * h) * 4; float y[8], s = 0, v = 0;
#pragma unroll
    for (int e = 0; e < 2; ++e) { const f32x4 ga = ldsf4(ma + 4096 + e * 32), ca = ldsf4(ma + 8192 + e * 32);
      x1p[2 * e] = pk2(lo16(xs[2 * e]) + ga[0] * r[4 * e], hi16(xs[2 * e]) + ga[1] * r[4 * e + 1]);
      x1p[2 * e + 1] = pk2(lo16(xs[2 * e + 1]) + ga[2] * r[4 * e + 2], hi16(xs[2 * e + 1]) + ga[3] * r[4 * e + 3]);
#pragma unroll
      for (int i = 0; i < 4; ++i) { r[4 * e + i] = (i & 1) ? hi16(x1p[2 * e + (i >> 1)]) : lo16(x1p[2 * e + (i >> 1)]); y[4 * e + i] = r[4 * e + i] * ca[i]; s += r[4 * e + i]; } }
    __builtin_amdgcn_raw_buffer_store_b128(pk8(y), rs, WS_X1 + ((Mt * 2 + wm2) * 32 + Nt * 4 + wq) * 1024 + lane * 16, 0, 16);
    s = hsum(s) * (1.f / 16.f);
#pragma unroll
    for (int i = 0; i < 8; ++i) v += (r[i] - s) * (r[i] - s);
    v = hsum(v);
    if (h == 0) *(LAS f32x2*)(L0 + L_SCR + wave * 256 + r32 * 8) = f32x2{s, v};
    bar();
    if (tid < 64) { f32x2 a[4];
#pragma unroll
      for (int i = 0; i < 4; ++i) a[i] = *(LAS f32x2*)(L0 + L_SCR + i * 512 + tid * 8);
      const float m = ((a[0][0] + a[1][0]) + (a[2][0] + a[3][0])) * 0.25f; float M = 0;
#pragma unroll
      for (int i = 0; i < 4; ++i) M += a[i][1] + 16.f * (a[i][0] - m) * (a[i][0] - m);
      __builtin_amdgcn_raw_buffer_store_b64(u32x2{__float_as_uint(m), __float_as_uint(M)}, rs, WS_ST + (((Mt * 2 + h) * 4 + (Nt >> 1)) * 32 + r32) * 16 + (Nt & 1) * 8, 0, 16); }
    ST(26);
    publish(ws, 512 + Mt * 8 + Nt, tag); }
  ST(8);
  load_wd(0, 6);
  const unsigned wb3 = L0 + 49152 + (4 * h + q) * 192 + cb * 32 + p * 8;
  if (wave >= 2) { f32x16 d = Z16; u32x4 fa[16], fb[16];
    const lp wpt = opq(wb3 + (wq - 1) * 64 + wm2 * 49152), mp = opq(L0 + L_MOD + (r32 == 1) * 1024 + wm2 * 512 + h * 16);
#pragma unroll
    for (int s = 0; s < 16; ++s) { fa[s] = *(LAS u32x4*)(mp + s * 32); fb[s] = fragp(wpt, s * 3072, 1536); }
    SB0();
#pragma unroll
    for (int s = 0; s < 16; ++s) d = mfma(fa[s], fb[s], d);
    if (h == 0) *(LAS f32x2*)(L0 + L_SCR + (wave - 2) * 256 + r32 * 8) = f32x2{d[0], d[1]}; }
  ST(28);
  wait_flags(ws, 512 + m3 * 32, 1, 32, tag, wave, lane);
  ST(9);
  { const int tt = m3 * 8 + wave;
#pragma unroll
    for (int s = 0; s < 32; ++s) xa[s] = __builtin_amdgcn_raw_buffer_load_b128(rs, WS_X1 + (tt * 32 + s) * 1024 + lane * 16, 0, 16);
    const f32x4 s0 = __builtin_bit_cast(f32x4, __builtin_amdgcn_raw_buffer_load_b128(rs, WS_ST + ((tt * 4 + 2 * h) * 32 + r32) * 16, 0, 16));
    const f32x4 s1 = __builtin_bit_cast(f32x4, __builtin_amdgcn_raw_buffer_load_b128(rs, WS_ST + ((tt * 4 + 2 * h + 1) * 32 + r32) * 16, 0, 16));
    unsigned uv[3][3];
#pragma unroll
    for (int T = 0; T < 3; ++T) { const f32x2 a = *(LAS f32x2*)(L0 + L_SCR + T * 512 + r32 * 8) + *(LAS f32x2*)(L0 + L_SCR + T * 512 + 256 + r32 * 8);
      const float uh = rbf(a[0]), vh = rbf(a[1]); uv[T][0] = pk2(uh, uh); uv[T][1] = pk2(a[0] - uh, vh); uv[T][2] = pk2(vh, a[1] - vh); }
    acc[0] = Z16; acc[1] = Z16; acc[2] = Z16;
    const lp wpp[2] = {opq(wb3), opq(wb3 + 49152)}; u32x4 w[2][3];
    auto ldw = [&](int s) {
#pragma unroll
      for (int c = 0; c < 3; ++c) w[s & 1][c] = fragp(wpp[s >> 4], (s & 15) * 3072 + c * 64, 1536); };
    ST2(21);
    ldw(0); SB0();
#pragma unroll
    for (int s = 0; s < 32; ++s) {
      if (s == 15) bar();
      if (s == 26) load_wd(6, 12);
      if (s < 31) ldw(s + 1);
#pragma unroll
      for (int c = 0; c < 3; ++c) acc[c] = mfma(w[s & 1][c], xa[s], acc[c]);
      ilv<3, 2, 0>(); SB0();
    }
    ST(10);
    const float mean = hsum((s0[0] + s0[2]) + (s1[0] + s1[2])) * 0.125f;
    const float d0 = s0[0] - mean, d1 = s0[2] - mean, d2 = s1[0] - mean, d3 = s1[2] - mean;
    const float var = hsum((s0[1] + s0[3]) + (s1[1] + s1[3]) + 64.f * (d0 * d0 + d1 * d1 + d2 * d2 + d3 * d3)) * (1.f / 512.f);
    const float rstd = rsqrtf(var + 1e-5f), sd = (var + 1e-5f) * rstd, ah = rbf(-mean), dh = rbf(sd), cg = -1.4426950408889634f * rstd;
    const u32x4 bx = {h ? 0u : pk2(ah, -mean - ah), h ? 0u : pk2(ah, dh), h ? 0u : pk2(sd - dh, dh), 0u};
#pragma unroll
    for (int T = 0; T < 3; ++T) acc[T] = mfma(u32x4{uv[T][0], uv[T][1], uv[T][2], 0u}, bx, acc[T]);
#pragma unroll
    for (int T = 0; T < 3; ++T) { float r[8];
#pragma unroll
      for (int i = 0; i < 8; ++i) r[i] = (acc[T][8 + i] * rstd) * __builtin_amdgcn_rcpf(1.f + __builtin_amdgcn_exp2f(cg * acc[T][i]));
      if (ul + T >= u0 && ul + T < u0 + nu) __builtin_amdgcn_raw_buffer_store_b128(pk8(r), rs, WS_H + (tt * 86 + ul + T) * 1024 + lane * 16, 0, 16);
    }
    ST(29);
    publish(ws, 768 + m3 * 32 + nf, tag); }
  ST(11);
  load_wd(12, 18);
  wait_flags(ws, 768 + (Mt >> 2) * 32, 1, 32, tag, wave, lane);
  ST(12);
  { const unsigned hb = WS_H + ((Mt * 2 + wm2) * 86 + ks4) * 1024 + lane * 16;
#pragma unroll
    for (int i = 0; i < 21; ++i) xa[i] = __builtin_amdgcn_raw_buffer_load_b128(rs, hb + i * 1024, 0, 16);
    xa[21] = __builtin_amdgcn_raw_buffer_load_b128(rs, hb + (wq < 2 ? 21 : 20) * 1024, 0, 16);
    acc[0] = Z16; acc[1] = Z16;
    lp wpp[3][2]; u32x4 w[2][2];
#pragma unroll
    for (int i = 0; i < 6; ++i) wpp[i >> 1][i & 1] = opq((wb8 ^ ((i & 1) * 64)) + (i >> 1) * 49152);
    auto ldw = [&](int i) { const int sl = i < 18 ? i : i - 18; w[i & 1][0] = fragp(wpp[sl / 6][0], (sl % 6) * 8192, 1024); w[i & 1][1] = fragp(wpp[sl / 6][1], (sl % 6) * 8192, 1024); };
    ST2(23);
    ldw(0); SB0();
#pragma unroll
    for (int i = 0; i < 22; ++i) {
      if (i == 3) { asm volatile("s_waitcnt vmcnt(22)" ::: "memory"); bar(); load_wd(18, 22); }
      if (i == 17) { wait_vm0(); bar(); }
      if (i < 21) ldw(i + 1);
      if (i == 21 && wq >= 2) xa[21] = u32x4{0, 0, 0, 0};
      acc[0] = mfma(w[i & 1][0], xa[i], acc[0]); acc[1] = mfma(w[i & 1][1], xa[i], acc[1]);
      ilv<2, 2, 0>(); SB0();
    }
    ST(13);
    float r[8]; reduce4(L0 + 49152, acc, wave, lane, r);
    unsigned o[4];
#pragma unroll
    for (int e = 0; e < 2; ++e) { const f32x4 gb = ldsf4(L0 + L_MOD + (2560 + col2 + 4 * h) * 4 + e * 32);
      o[2 * e] = pk2(lo16(x1p[2 * e]) + gb[0] * r[4 * e], hi16(x1p[2 * e]) + gb[1] * r[4 * e + 1]);
      o[2 * e + 1] = pk2(lo16(x1p[2 * e + 1]) + gb[2] * r[4 * e + 2], hi16(x1p[2 * e + 1]) + gb[3] * r[4 * e + 3]); }
    swap32(o[0], o[2]); swap32(o[1], o[3]);
    *(u32x4*)(out + tok2 * 512 + col2 + 8 * h) = u32x4{o[0], o[1], o[2], o[3]};
  }
  ST(14);
}

extern "C" void kernel_launch(void* const* d_in, const int* in_sizes, int n_in, void* d_out, int out_size, void* d_ws, size_t ws_size,
                              hipStream_t stream) {
  typedef const unsigned short* P;
  adaln_block<<<256, 512, 0, stream>>>((P)d_in[0], (P)d_in[1], (P)d_in[2], (P)d_in[3], (P)d_in[5], (P)d_in[6], (P)d_in[7], (P)d_in[8], (P)d_in[9],
                                       (unsigned short*)d_out, (char*)d_ws);
}
```

```cpp
#include <hip/hip_runtime.h>
#include <hip/hip_bf16.h>
#include <type_traits>

typedef __attribute__((ext_vector_type(8))) __bf16 bf16x8;
typedef __attribute__((ext_vector_type(2))) __bf16 bf16x2;
typedef __attribute__((ext_vector_type(4))) short s16x4;
typedef __attribute__((ext_vector_type(4))) float f32x4;
typedef __attribute__((ext_vector_type(2))) float f32x2;
typedef __attribute__((ext_vector_type(16))) float f32x16;
typedef __attribute__((ext_vector_type(4))) unsigned u32x4;
typedef __attribute__((ext_vector_type(2))) unsigned u32x2;
typedef __attribute__((address_space(1))) unsigned gu32;
typedef __attribute__((address_space(1))) unsigned long long gu64;
#define LAS __attribute__((address_space(3)))
#define RLX_AGENT __ATOMIC_RELAXED, __HIP_MEMORY_SCOPE_AGENT
#define DEV __device__ __forceinline__

constexpr unsigned WS_FLAG = 4096;
constexpr unsigned WS_MOD = WS_FLAG + 4 * 256 * 128;
constexpr unsigned WS_KV = WS_MOD + 16384;
constexpr unsigned WS_ST = WS_KV + 256 * 4352;
constexpr unsigned WS_O = WS_ST + 2048 * 128;
constexpr unsigned WS_X1 = WS_O + 2097152;
constexpr unsigned WS_H = WS_X1 + 2097152;
constexpr unsigned WS_END = WS_H + 128 * 43 * 1024;
constexpr unsigned L_MOD = 147456, L_SCR = 159744;
constexpr int D3 = 10, D4 = 12;
constexpr int CODE_PF = 32768;
constexpr unsigned SPIN_CAP = 1u << 19;

DEV unsigned pk2(float a, float b) { f32x2 v = {a, b}; return __builtin_bit_cast(unsigned, __builtin_convertvector(v, bf16x2)); }
DEV float lo16(unsigned u) { return __uint_as_float(u << 16); }
DEV float hi16(unsigned u) { return __uint_as_float(u & 0xffff0000u); }
DEV float rbf(float a) { return lo16(pk2(a, a)); }
DEV f32x16 mfma(u32x4 a, u32x4 b, f32x16 c) {
  return __builtin_amdgcn_mfma_f32_32x32x16_bf16(__builtin_bit_cast(bf16x8, a), __builtin_bit_cast(bf16x8, b), c, 0, 0, 0); }
DEV u32x4 pk8(const float* v) { return u32x4{pk2(v[0], v[1]), pk2(v[2], v[3]), pk2(v[4], v[5]), pk2(v[6], v[7])}; }
DEV void swap32(unsigned& a, unsigned& b) { auto s = __builtin_amdgcn_permlane32_swap(a, b, false, false); a = s[0]; b = s[1]; }
DEV float hsum(float x) { unsigned a = __float_as_uint(x), b = a; swap32(a, b); return __uint_as_float(a) + __uint_as_float(b); }
DEV float hmax(float x) { unsigned a = __float_as_uint(x), b = a; swap32(a, b); return fmaxf(__uint_as_float(a), __uint_as_float(b)); }
#define QSEL(o0, o1, mv0, keep0, mv1, keep1, MASK, QP) asm("s_mov_b32 vcc_lo, " MASK "\n\ts_mov_b32 vcc_hi, " MASK "\n\ts_nop 1\n\t" \
  "v_cndmask_b32_dpp %0, %8, %16, vcc " QP "\n\tv_cndmask_b32_dpp %1, %9, %17, vcc " QP "\n\tv_cndmask_b32_dpp %2, %10, %18, vcc " QP "\n\tv_cndmask_b32_dpp %3, %11, %19, vcc " QP "\n\t" \
  "v_cndmask_b32_dpp %4, %12, %20, vcc " QP "\n\tv_cndmask_b32_dpp %5, %13, %21, vcc " QP "\n\tv_cndmask_b32_dpp %6, %14, %22, vcc " QP "\n\tv_cndmask_b32_dpp %7, %15, %23, vcc " QP \
  : "=&v"(o0[0]), "=&v"(o0[1]), "=&v"(o0[2]), "=&v"(o0[3]), "=&v"(o1[0]), "=&v"(o1[1]), "=&v"(o1[2]), "=&v"(o1[3]) \
  : "v"(mv0[0]), "v"(mv0[1]), "v"(mv0[2]), "v"(mv0[3]), "v"(mv1[0]), "v"(mv1[1]), "v"(mv1[2]), "v"(mv1[3]), \
    "v"(keep0[0]), "v"(keep0[1]), "v"(keep0[2]), "v"(keep0[3]), "v"(keep1[0]), "v"(keep1[1]), "v"(keep1[2]), "v"(keep1[3]) : "vcc")
#define QX1 "quad_perm:[1,0,3,2] row_mask:0xf bank_mask:0xf"
#define QX2 "quad_perm:[2,3,0,1] row_mask:0xf bank_mask:0xf"
#define XRED(name, OP) DEV float name(float x) { \
  auto s = __builtin_amdgcn_permlane16_swap(__float_as_uint(x), __float_as_uint(x), false, false); \
  x = OP(__uint_as_float(s[0]), __uint_as_float(s[1])); \
  auto t = __builtin_amdgcn_permlane32_swap(__float_as_uint(x), __float_as_uint(x), false, false); \
  return OP(__uint_as_float(t[0]), __uint_as_float(t[1])); }
DEV float addf(float a, float b) { return a + b; }
XRED(xsum, addf)
template <int N> DEV float ror(float x) { return __int_as_float(__builtin_amdgcn_update_dpp(0, __float_as_int(x), 0x120 + N, 0xf, 0xf, false)); }

DEV void glds(const void* g, unsigned lds) {
  unsigned keep;
  asm volatile("s_mov_b32 %0, m0\n\ts_mov_b32 m0, %2\n\ts_nop 0\n\tglobal_load_lds_dwordx4 %1, off\n\ts_mov_b32 m0, %0"
               : "=&s"(keep) : "v"(g), "s"(lds) : "memory");
}
DEV void wait_vm0() { asm volatile("s_waitcnt vmcnt(0)" ::: "memory"); }
DEV void bar() { asm volatile("s_waitcnt lgkmcnt(0)\n\ts_barrier" ::: "memory"); }
typedef LAS char* lp;
DEV lp opq(unsigned a) { asm volatile("" : "+v"(a)); return (lp)a; }
DEV u32x4 fragp(lp a, int o, int d) {
  u32x2 x = __builtin_bit_cast(u32x2, __builtin_amdgcn_ds_read_tr16_b64_v4i16((LAS s16x4*)(a + o)));
  u32x2 y = __builtin_bit_cast(u32x2, __builtin_amdgcn_ds_read_tr16_b64_v4i16((LAS s16x4*)(a + o + d)));
  return u32x4{x[0], x[1], y[0], y[1]}; }
DEV f32x4 ldp4(lp a, int o) { return *(LAS f32x4*)(a + o); }
template <int I, int N, class F> DEV void sfor(F f) { if constexpr (I < N) { f(std::integral_constant<int, I>{}); sfor<I + 1, N>(f); } }
#define SB0() __builtin_amdgcn_sched_barrier(0)
template <int NM, int ND, int NV> DEV void ilv() {
#pragma unroll
  for (int i = 0; i < NM; ++i) { __builtin_amdgcn_sched_group_barrier(0x008, 1, 0); __builtin_amdgcn_sched_group_barrier(0x100, ND, 0);
    if (NV) __builtin_amdgcn_sched_group_barrier(0x002, NV, 0); }
}
DEV f32x4 ldsf4(unsigned a) { return *(LAS f32x4*)a; }
DEV void stsf4(unsigned a, f32x4 v) { *(LAS f32x4*)a = v; }

DEV void wait_flags(char* ws, unsigned first, unsigned stride, unsigned n, unsigned tag, int wave, int lane) {
  if (wave == 0) {
    gu32* p = (gu32*)(ws + WS_FLAG) + (first + (lane & (n - 1)) * stride) * 32;
#pragma unroll 1
    for (unsigned s = 0; s < SPIN_CAP; ++s) {
      if (__all(__hip_atomic_load(p, RLX_AGENT) == tag)) break;
      __builtin_amdgcn_s_sleep(1);
    }
  }
  bar();
}
DEV void publish(char* ws, unsigned idx, unsigned tag) {
  wait_vm0(); bar();
  if (threadIdx.x == 0) __hip_atomic_store((gu32*)(ws + WS_FLAG) + idx * 32, tag, RLX_AGENT);
}
DEV u32x4 ldx(__amdgpu_buffer_rsrc_t rs, unsigned v, unsigned s) { return __builtin_amdgcn_raw_buffer_load_b128(rs, v, s, 16); }
#define ST(i)
#define ST2(i)

DEV void hedgehog(u32x4 wp, const float* v, u32x4& o0, u32x4& o1) {
  const float L = 1.4426950408889634f; f32x16 Z = {0}; f32x16 pp = mfma(wp, pk8(v), Z); float e[16];
#pragma unroll
  for (int s = 0; s < 2; ++s) {
    float m = fmaxf(fmaxf(fmaxf(pp[8 * s], pp[8 * s + 1]), fmaxf(pp[8 * s + 2], pp[8 * s + 3])), fmaxf(fmaxf(pp[8 * s + 4], pp[8 * s + 5]), fmaxf(pp[8 * s + 6], pp[8 * s + 7])));
    m = -hmax(m) * L; float t = 0;
#pragma unroll
    for (int i = 0; i < 8; ++i) { e[8 * s + i] = __builtin_amdgcn_exp2f(pp[8 * s + i] * L + m); t += e[8 * s + i]; }
    t = __builtin_amdgcn_rcpf(hsum(t));
#pragma unroll
    for (int i = 0; i < 8; ++i) e[8 * s + i] *= t;
  }
  o0 = pk8(e); o1 = pk8(e + 8);
}
DEV void reduce4(unsigned base, const f32x16 (&acc)[3], int wave, int lane, float* r) {
  const int wm = wave & 1, wq = wave >> 1;
#pragma unroll
  for (int d = 0; d < 4; ++d) {
    const f32x16 v = acc[d >> 1]; const int o = (d & 1) * 8;
    if (d != wq) { const unsigned a = base + ((d * 2 + wm) * 3 + ((wq - d - 1) & 3)) * 2048 + lane * 16;
      stsf4(a, f32x4{v[o], v[o + 1], v[o + 2], v[o + 3]}); stsf4(a + 1024, f32x4{v[o + 4], v[o + 5], v[o + 6], v[o + 7]}); }
    else {
#pragma unroll
      for (int i = 0; i < 8; ++i) r[i] = v[o + i]; }
  }
  bar();
#pragma unroll
  for (int s = 0; s < 6; ++s) { const f32x4 v = ldsf4(base + wave * 6144 + s * 1024 + lane * 16);
#pragma unroll
    for (int i = 0; i < 4; ++i) r[(s & 1) * 4 + i] += v[i]; }
}

__global__ void __launch_bounds__(512, 2)
adaln_block(const unsigned short* __restrict__ x, const unsigned short* __restrict__ wqkv, const unsigned short* __restrict__ wphi,
            const unsigned short* __restrict__ wo, const unsigned short* __restrict__ wg, const unsigned short* __restrict__ wu,
            const unsigned short* __restrict__ wd, const unsigned short* __restrict__ cin, const unsigned short* __restrict__ wmod,
            unsigned short* __restrict__ out, char* ws) {
  __shared__ __attribute__((aligned(16))) char smem[163840];
  const unsigned L0 = (unsigned)(unsigned long long)(LAS char*)smem;
  const int tid = threadIdx.x, lane = tid & 63, wave = __builtin_amdgcn_readfirstlane(tid >> 6);
  const int h = lane >> 5, r32 = lane & 31, cb = (lane >> 4) & 1, q = (lane & 15) >> 2, p = lane & 3;
  const int b = blockIdx.x, xc = b & 7, j = b >> 3;
  const int mt = xc * 2 + (j & 1), nt = j >> 1;
  const int Mt = xc * 4 + (j & 3), Nt = j >> 2;
  const int m3 = (xc >> 1) * 2 + (j & 1), nf = (xc & 1) * 16 + (j >> 1);
  const int wm = wave & 3, wk = wave >> 2, wm2 = wave & 1, wq = wave >> 1;
  const __amdgpu_buffer_rsrc_t rs = __builtin_amdgcn_make_buffer_rsrc(ws, 0, WS_END, 0x00020000);
  const unsigned wbase = L0 + wave * 1024;
  const f32x16 Z16 = {0};
  ST(0);
  unsigned pf0 = 0;
  if (wave >= 6) {
    const char* pc = (const char*)__builtin_amdgcn_s_getpc() + (((j & 1) * 2 + (wave & 1)) * (CODE_PF / 4) + lane * (CODE_PF / 256));
    asm volatile("global_load_dword %0, %1, off" : "=v"(pf0) : "v"(pc) : "memory"); }
  unsigned tagv = __hip_atomic_load((gu32*)ws, RLX_AGENT);

  u32x4 wv[4]; unsigned short cv[4]; const int gb = xc * 12 + j;
  if (j < 12) {
#pragma unroll
    for (int i = 0; i < 4; ++i) { int row = (tid >> 2) + 128 * i; wv[i] = *(const u32x4*)(wmod + row * 3072 + gb * 32 + p * 8); cv[i] = cin[row]; } }
  u32x4 xa[32];
  { const unsigned short* xp = x + (mt * 128 + wm * 32 + (r32 & 28)) * 512 + wk * 256 + h * 32 + p * 8;
#pragma unroll
    for (int i = 0; i < 16; ++i) { xa[(i & 3) * 4 + (i >> 2)] = *(const u32x4*)(xp + (i >> 2) * 512 + (i & 3) * 64); SB0(); } }
  u32x4 wp;
  { const unsigned short* pp = wphi + (nt * 2 + wk) * 256 + 4 * h * 16 + (lane & 15); const unsigned sg = cb ? 0x80008000u : 0u;
#pragma unroll
    for (int e = 0; e < 4; ++e) { const int d = (e >> 1) * 8 + (e & 1) * 2; wp[e] = ((unsigned)pp[d * 16] | ((unsigned)pp[d * 16 + 16] << 16)) ^ sg; } }
  const int u0 = nf < 22 ? 3 * nf : 2 * nf + 22, nu = nf < 22 ? 3 : 2, ul = u0 > 83 ? 83 : u0;
  const unsigned short *gsrc[3], *qsrc[3];
#pragma unroll
  for (int i = 0; i < 3; ++i) {
    const int c0 = i * 512 + tid, row = c0 / 12, cc = c0 - row * 12;
    qsrc[i] = wqkv + row * 1536 + (cc >> 2) * 512 + nt * 32 + (cc & 3) * 8;
  }
  auto load_gu = [&](int n0, int n1) {
#pragma unroll
    for (int n = n0; n < n1; ++n) glds(gsrc[n % 3] + (n / 3) * 128 * 1376, wbase + 49152 + n * 8192); };
  const int off8 = (wm2 * 8 + (lane >> 3)) * 512 + Nt * 64 + ((((lane >> 2) ^ (lane >> 4)) & 1) * 32) + p * 8;
  const int ks4 = wq * 22 - (wq == 3);
  auto load_wd = [&](int i0, int i1) {
#pragma unroll
    for (int i = i0; i < i1; ++i) glds(wd + off8 + (i == 21 && wq == 3 ? 85 : ks4 + i) * 8192, wbase + (i < 18 ? i : i - 18) * 8192); };

  auto load_wo = [&](int i0, int i1) {
#pragma unroll
    for (int i = i0; i < i1; ++i) glds(wo + off8 + (wq * 8 + i) * 8192, wbase + (i < 6 ? 98304 : 0) + i * 8192); };
  SB0(); asm volatile("" : "+v"(tagv));
  const unsigned tag = __builtin_amdgcn_readfirstlane(tagv) + 1u;
  if (j < 12) {
    f32x2 a[4] = {{0, 0}, {0, 0}, {0, 0}, {0, 0}};
#pragma unroll
    for (int i = 0; i < 4; ++i) {
      float c = lo16(cv[i]); c = rbf(c * __builtin_amdgcn_rcpf(1.f + __expf(-c)));
#pragma unroll
      for (int e = 0; e < 4; ++e) a[e] += c * f32x2{lo16(wv[i][e]), hi16(wv[i][e])};
    }
#pragma unroll
    for (int e = 0; e < 8; ++e) { float v = a[e >> 1][e & 1]; v += ror<4>(v); v += ror<8>(v); a[e >> 1][e & 1] = v; }
    if (q == 0) { const unsigned d = L0 + L_MOD + (tid >> 4) * 128 + p * 32;
      stsf4(d, f32x4{a[0][0], a[0][1], a[1][0], a[1][1]}); stsf4(d + 16, f32x4{a[2][0], a[2][1], a[3][0], a[3][1]}); }
    bar();
    if (wave == 0) { f32x2 s = {0, 0};
#pragma unroll
      for (int w = 0; w < 8; ++w) s += *(LAS f32x2*)(L0 + L_MOD + w * 512 + lane * 8);
      s[0] = xsum(s[0]); s[1] = xsum(s[1]);
      if (lane < 16) __hip_atomic_store((gu64*)(ws + WS_MOD) + gb * 16 + lane, ((unsigned long long)tag << 32) | pk2(s[0], s[1]), RLX_AGENT); } }
  asm volatile("" : "+v"(xa[0]), "+v"(xa[1]), "+v"(xa[2]), "+v"(xa[3]), "+v"(xa[4]), "+v"(xa[5]), "+v"(xa[6]), "+v"(xa[7]), "+v"(xa[8]), "+v"(xa[9]),
               "+v"(xa[10]), "+v"(xa[11]), "+v"(xa[12]), "+v"(xa[13]), "+v"(xa[14]), "+v"(xa[15]), "+v"(wp), "+v"(pf0));
  ST(15);
  f32x2 t[64]; u32x4 g[4];
  auto ldg = [&]() {
#pragma unroll
    for (int i = 0; i < 4; ++i) g[i] = ldx(rs, WS_MOD + (wk * 256 + q * 64 + h * 32 + p * 8) * 4 + (i >> 1) * 2048 + (i & 1) * 16, 0); };
  { f32x2 sv = {0, 0}, vv = {0, 0};
#pragma unroll
    for (int s = 0; s < 4; ++s) { unsigned n[4][4], m[4][4]; const u32x4 r0 = xa[4 * s], r1 = xa[4 * s + 1], r2 = xa[4 * s + 2], r3 = xa[4 * s + 3];
#pragma unroll
      for (int i = 0; i < 3; ++i) glds(qsrc[i] + s * 128 * 1536, wbase + (s * 3 + i) * 8192);
      SB0();
      QSEL(n[1], n[3], r0, r1, r2, r3, "0xaaaaaaaa", QX1); QSEL(n[0], n[2], r1, r0, r3, r2, "0x55555555", QX1);
      QSEL(m[2], m[3], n[0], n[2], n[1], n[3], "0xcccccccc", QX2); QSEL(m[0], m[1], n[2], n[0], n[3], n[1], "0x33333333", QX2);
#pragma unroll
      for (int i = 0; i < 16; ++i) { t[16 * s + i] = f32x2{lo16(m[i >> 2][i & 3]), hi16(m[i >> 2][i & 3])}; sv += t[16 * s + i]; vv += t[16 * s + i] * t[16 * s + i]; }
      SB0(); }
    ST(16);
    const float sh = hsum(sv[0] + sv[1]), vh = hsum(vv[0] + vv[1]);
    if (h == 0) *(LAS f32x2*)(L0 + L_SCR + 1024 + wave * 256 + r32 * 8) = f32x2{sh, vh};
    bar();
    ldg(); SB0();
    const f32x2 o = *(LAS f32x2*)(L0 + L_SCR + 1024 + (wave ^ 4) * 256 + r32 * 8);
    const float mean = (sh + o[0]) * (1.f / 512.f), rstd = rsqrtf(fmaxf((vh + o[1]) * (1.f / 512.f) - mean * mean, 0.f) + 1e-5f), dl = -mean * rstd;
#pragma unroll
    for (int i = 0; i < 64; ++i) { t[i] = t[i] * rstd + dl; asm volatile("" : "+v"(t[i])); } }
  ST(1);
  float ms[8], mc[8];
  {
#pragma unroll 1
    for (unsigned s = 0; s < SPIN_CAP; ++s) { bool ok = true;
#pragma unroll
      for (int i = 0; i < 4; ++i) ok = ok && g[i][1] == tag && g[i][3] == tag;
      if (__all(ok)) break;
      asm volatile("s_sleep 1" ::: "memory");
      ldg();
    }
#pragma unroll
    for (int i = 0; i < 4; ++i) { const unsigned u = g[i >> 1][(i & 1) * 2], v = g[2 + (i >> 1)][(i & 1) * 2], c = pk2(1.f + lo16(v), 1.f + hi16(v));
      ms[2 * i] = lo16(u); ms[2 * i + 1] = hi16(u); mc[2 * i] = lo16(c); mc[2 * i + 1] = hi16(c); } }
  wait_vm0(); bar();
  ST(2);
  load_wo(0, 6);
  f32x16 acc[3] = {Z16, Z16, Z16};
  { const lp wb = opq(L0 + (wk * 256 + h * 32 + q) * 192 + cb * 32 + p * 8);
    u32x4 w[2][3], hh[2];
    auto ldw = [&](int st, int c) { w[st & 1][c] = fragp(wb, ((st >> 2) * 64 + (st & 3) * 8) * 192 + c * 64, 768); };
    auto mkh = [&](auto S, int e) {
      constexpr int st = S; float v0 = t[st * 4 + e][0], v1 = t[st * 4 + e][1];
      asm("v_mul_f32_dpp %0, %2, %0 row_newbcast:%6 row_mask:0xf bank_mask:0xf\n\tv_mul_f32_dpp %1, %3, %1 row_newbcast:%6 row_mask:0xf bank_mask:0xf\n\t"
          "v_add_f32_dpp %0, %4, %0 row_newbcast:%6 row_mask:0xf bank_mask:0xf\n\tv_add_f32_dpp %1, %5, %1 row_newbcast:%6 row_mask:0xf bank_mask:0xf"
          : "+v"(v0), "+v"(v1) : "v"(mc[2 * e]), "v"(mc[2 * e + 1]), "v"(ms[2 * e]), "v"(ms[2 * e + 1]), "i"(st));
      hh[st & 1][e] = pk2(v0, v1); };
#pragma unroll
    for (int e = 0; e < 8; ++e) asm volatile("" : "+v"(ms[e]), "+v"(mc[e]));
    asm volatile("s_nop 1"); SB0();
    ldw(0, 0); ldw(0, 1); ldw(0, 2);
#pragma unroll
    for (int e = 0; e < 4; ++e) mkh(std::integral_constant<int, 0>{}, e);
    SB0();
    sfor<0, 16>([&](auto S) { constexpr int st = S; std::integral_constant<int, (st + 1) & 15> N;
      acc[0] = mfma(w[st & 1][0], hh[st & 1], acc[0]); SB0();
      if (st < 15) { ldw(st + 1, 0); mkh(N, 0); mkh(N, 1); }
      SB0(); acc[1] = mfma(w[st & 1][1], hh[st & 1], acc[1]); SB0();
      if (st < 15) { ldw(st + 1, 1); mkh(N, 2); }
      SB0(); acc[2] = mfma(w[st & 1][2], hh[st & 1], acc[2]); SB0();
      if (st < 15) { ldw(st + 1, 2); mkh(N, 3); }
      SB0(); }); }
  ST(3);
  u32x4 phq0, phq1, gm;
  { float v[3][8];
    bar(); load_wo(6, 8);
    auto send = [&](int o) {
#pragma unroll
      for (int s = 0; s < 6; ++s) { const f32x16 a = acc[s >> 1]; const int i = o + (s & 1) * 4; stsf4(L0 + wave * 6144 + s * 1024 + lane * 16, f32x4{a[i], a[i + 1], a[i + 2], a[i + 3]}); } };
    auto recv = [&](int o) {
#pragma unroll
      for (int s = 0; s < 6; ++s) { const f32x4 r = ldsf4(L0 + (wave ^ 4) * 6144 + s * 1024 + lane * 16);
#pragma unroll
        for (int i = 0; i < 4; ++i) v[s >> 1][(s & 1) * 4 + i] = acc[s >> 1][o + (s & 1) * 4 + i] + r[i]; } };
    if (wk) { send(0); bar(); recv(8); } else { send(8); bar(); recv(0); }
    ST(17);
    u32x4 k0, k1; hedgehog(wp, v[1], k0, k1);
    const u32x4 vb = pk8(v[2]);
    const unsigned img = L0 + 65536 + wave * 4096, wr = img + r32 * 104 + h * 8;
    *(LAS u32x2*)(wr) = u32x2{k0[0], k0[1]}; *(LAS u32x2*)(wr + 16) = u32x2{k0[2], k0[3]}; *(LAS u32x2*)(wr + 32) = u32x2{k1[0], k1[1]}; *(LAS u32x2*)(wr + 48) = u32x2{k1[2], k1[3]};
    *(LAS u32x2*)(wr + 64) = u32x2{vb[0], vb[1]}; *(LAS u32x2*)(wr + 80) = u32x2{vb[2], vb[3]};
    asm volatile("" ::: "memory");
    f32x16 kv = Z16; const lp ra = opq(img + (8 * h + q) * 104 + cb * 32 + p * 8), rb = opq(img + (8 * h + q) * 104 + 64 + p * 8);
#pragma unroll
    for (int s = 0; s < 2; ++s) { u32x4 bb = fragp(rb, s * 1664, 416);
#pragma unroll
      for (int e = 0; e < 4; ++e) bb[e] = cb ? 0x3f803f80u : bb[e];
      kv = mfma(fragp(ra, s * 1664, 416), bb, kv); }
    asm volatile("" ::: "memory");
    ST(18);
    if (r32 <= 16) {
#pragma unroll
      for (int c = 0; c < 4; ++c) stsf4(img + (c * 34 + h * 17 + r32) * 16, f32x4{kv[4 * c], kv[4 * c + 1], kv[4 * c + 2], kv[4 * c + 3]}); }
    bar();
    if (tid < 272) { const unsigned a = L0 + 65536 + (tid < 136 ? tid * 16 : 16384 + (tid - 136) * 16);
      const f32x4 s = (ldsf4(a) + ldsf4(a + 4096)) + (ldsf4(a + 8192) + ldsf4(a + 12288));
      __builtin_amdgcn_raw_buffer_store_b128(__builtin_bit_cast(u32x4, s), rs, WS_KV + (nt * 16 + mt) * 4352 + tid * 16, 0, 16); }
    ST(20);
    publish(ws, mt * 16 + nt, tag);
    ST(4);
    gm = ldx(rs, WS_MOD + 4096 + tid * 16, 0);
    int tid2 = tid; asm volatile("" : "+v"(tid2));
#pragma unroll
    for (int i = 0; i < 3; ++i) { const int c0 = i * 512 + tid2, row = c0 / 12, cc = c0 - row * 12;
      gsrc[i] = ((cc & 2) ? wu : wg) + row * 1376 + (ul + (cc >> 2)) * 16 + (cc & 1) * 8; }
    hedgehog(wp, v[0], phq0, phq1); }
  const int tok2 = Mt * 64 + wm2 * 32 + r32, col2 = Nt * 64 + wq * 16;
  const u32x4 xr = *(const u32x4*)(x + tok2 * 512 + col2 + 8 * h);
  const unsigned wb8 = L0 + wq * 2048 + (4 * h + q) * 128 + cb * 32 + p * 8 + (q >> 1) * 64;
  u32x4 wf[8][2];
  { const lp wa[4] = {opq(wb8 + 98304), opq((wb8 ^ 64) + 98304), opq(wb8), opq(wb8 ^ 64)};
#pragma unroll
    for (int i = 0; i < 16; ++i) wf[i >> 1][i & 1] = fragp(wa[(i & 1) + (i < 12 ? 0 : 2)], (i >> 1) * 8192, 1024); }
#pragma unroll 1
  for (unsigned s = 0; s < SPIN_CAP; ++s) {
    if (__all(gm[1] == tag && gm[3] == tag)) break;
    asm volatile("s_sleep 1" ::: "memory");
    gm = ldx(rs, WS_MOD + 4096 + tid * 16, 0);
  }
  if (wq == 2) { gm[0] = pk2(1.f + lo16(gm[0]), 1.f + hi16(gm[0])); gm[2] = pk2(1.f + lo16(gm[2]), 1.f + hi16(gm[2])); }
  stsf4(L0 + L_MOD + 4096 + tid * 16, f32x4{lo16(gm[0]), hi16(gm[0]), lo16(gm[2]), hi16(gm[2])});
  if (wq == 1 || wq == 2) *(LAS u32x2*)(L0 + L_MOD + (2 - wq) * 1024 + (tid & 124) * 8 + (tid & 1) * 16 + (tid & 2) * 4) = u32x2{gm[0], gm[2]};
  load_gu(2, 6);
  wait_flags(ws, nt, 16, 16, tag, wave, lane);
  ST(5);
  { if (tid < 272) { u32x4 v[16];
#pragma unroll
      for (int i = 0; i < 16; ++i) v[i] = __builtin_amdgcn_raw_buffer_load_b128(rs, WS_KV + (nt * 16 + i) * 4352 + tid * 16, 0, 16);
      f32x4 s = __builtin_bit_cast(f32x4, v[0]);
#pragma unroll
      for (int i = 1; i < 16; ++i) s += __builtin_bit_cast(f32x4, v[i]);
      *(LAS u32x2*)(L0 + L_SCR + tid * 8) = u32x2{pk2(s[0], s[1]), pk2(s[2], s[3])}; }
    bar();
    ST(22);
    const unsigned a = L0 + L_SCR + (wk * 136 + h * 17 + (r32 < 16 ? r32 : 16)) * 8;
    const u32x2 c0 = *(LAS u32x2*)a, c1 = *(LAS u32x2*)(a + 272), c2 = *(LAS u32x2*)(a + 544), c3 = *(LAS u32x2*)(a + 816);
    const f32x16 d = mfma(u32x4{c2[0], c2[1], c3[0], c3[1]}, phq1, mfma(u32x4{c0[0], c0[1], c1[0], c1[1]}, phq0, Z16));
    const float rd = __builtin_amdgcn_rcpf(d[8] + 1e-6f); float o[8];
#pragma unroll
    for (int i = 0; i < 8; ++i) o[i] = d[i] * rd;
    __builtin_amdgcn_raw_buffer_store_b128(pk8(o), rs, WS_O + ((mt * 4 + wm) * 32 + nt * 2 + wk) * 1024 + lane * 16, 0, 16);
    ST(24);
    publish(ws, 256 + mt * 16 + nt, tag); }
  ST(6);
  load_gu(0, 2); load_gu(6, 9);
  wait_flags(ws, 256 + (Mt >> 1) * 16, 1, 16, tag, wave, lane);
  if (b == 0 && tid == 0) __hip_atomic_store((gu32*)ws, tag, RLX_AGENT);
  ST(7);
  u32x4 x1p;
  {
#pragma unroll
    for (int i = 0; i < 8; ++i) { xa[i] = ldx(rs, lane * 16, WS_O + ((Mt * 2 + wm2) * 32 + wq * 8 + i) * 1024); SB0(); }
    acc[0] = Z16; acc[1] = Z16;
#pragma unroll
    for (int i = 0; i < 8; ++i) { acc[0] = mfma(wf[i][0], xa[i], acc[0]); acc[1] = mfma(wf[i][1], xa[i], acc[1]); SB0(); }
    load_gu(9, 12);
    ST(25);
    float r[8]; reduce4(L0, acc, wave, lane, r);
    unsigned xs[4] = {xr[0], xr[1], xr[2], xr[3]}; swap32(xs[0], xs[2]); swap32(xs[1], xs[3]);
    const unsigned ma = L0 + L_MOD + (col2 + 4 * h) * 4; float y[8], s = 0, v = 0;
#pragma unroll
    for (int e = 0; e < 2; ++e) { const f32x4 ga = ldsf4(ma + 4096 + e * 32), ca = ldsf4(ma + 8192 + e * 32);
      x1p[2 * e] = pk2(lo16(xs[2 * e]) + ga[0] * r[4 * e], hi16(xs[2 * e]) + ga[1] * r[4 * e + 1]);
      x1p[2 * e + 1] = pk2(lo16(xs[2 * e + 1]) + ga[2] * r[4 * e + 2], hi16(xs[2 * e + 1]) + ga[3] * r[4 * e + 3]);
#pragma unroll
      for (int i = 0; i < 4; ++i) { r[4 * e + i] = (i & 1) ? hi16(x1p[2 * e + (i >> 1)]) : lo16(x1p[2 * e + (i >> 1)]); y[4 * e + i] = r[4 * e + i] * ca[i]; s += r[4 * e + i]; } }
    __builtin_amdgcn_raw_buffer_store_b128(pk8(y), rs, WS_X1 + ((Mt * 2 + wm2) * 32 + Nt * 4 + wq) * 1024 + lane * 16, 0, 16);
    s = hsum(s) * (1.f / 16.f);
#pragma unroll
    for (int i = 0; i < 8; ++i) v += (r[i] - s) * (r[i] - s);
    v = hsum(v);
    if (h == 0) *(LAS f32x2*)(L0 + L_SCR + wave * 256 + r32 * 8) = f32x2{s, v};
    bar();
    if (tid < 64) { f32x2 a[4];
#pragma unroll
      for (int i = 0; i < 4; ++i) a[i] = *(LAS f32x2*)(L0 + L_SCR + i * 512 + tid * 8);
      const float m = ((a[0][0] + a[1][0]) + (a[2][0] + a[3][0])) * 0.25f; float M = 0;
#pragma unroll
      for (int i = 0; i < 4; ++i) M += a[i][1] + 16.f * (a[i][0] - m) * (a[i][0] - m);
      __builtin_amdgcn_raw_buffer_store_b64(u32x2{__float_as_uint(m), __float_as_uint(M)}, rs, WS_ST + (((Mt * 2 + h) * 4 + (Nt >> 1)) * 32 + r32) * 16 + (Nt & 1) * 8, 0, 16); }
    ST(26);
    publish(ws, 512 + Mt * 8 + Nt, tag); }
  ST(8);
  load_wd(0, 6);
  const unsigned wb3 = L0 + 49152 + (4 * h + q) * 192 + cb * 32 + p * 8;
  if (wave >= 2) { f32x16 d = Z16; u32x4 fa[16], fb[16];
    const lp wpt = opq(wb3 + (wq - 1) * 64 + wm2 * 49152), mp = opq(L0 + L_MOD + (r32 == 1) * 1024 + wm2 * 512 + h * 16);
#pragma unroll
    for (int s = 0; s < 16; ++s) { fa[s] = *(LAS u32x4*)(mp + s * 32); fb[s] = fragp(wpt, s * 3072, 1536); }
    SB0();
#pragma unroll
    for (int s = 0; s < 16; ++s) d = mfma(fa[s], fb[s], d);
    if (h == 0) *(LAS f32x2*)(L0 + L_SCR + (wave - 2) * 256 + r32 * 8) = f32x2{d[0], d[1]}; }
  ST(28);
  wait_flags(ws, 512 + m3 * 32, 1, 32, tag, wave, lane);
  ST(9);
  { const int tt = m3 * 8 + wave;
    auto lda = [&](int s) { xa[s] = ldx(rs, lane * 16, WS_X1 + (tt * 32 + s) * 1024); };
#pragma unroll
    for (int s = 0; s < D3; ++s) { lda(s); SB0(); }
    u32x4 s0u = ldx(rs, WS_ST + ((tt * 4 + 2 * h) * 32 + r32) * 16, 0), s1u = ldx(rs, WS_ST + ((tt * 4 + 2 * h + 1) * 32 + r32) * 16, 0);
    unsigned uv[3][3];
#pragma unroll
    for (int T = 0; T < 3; ++T) { const f32x2 a = *(LAS f32x2*)(L0 + L_SCR + T * 512 + r32 * 8) + *(LAS f32x2*)(L0 + L_SCR + T * 512 + 256 + r32 * 8);
      const float uh = rbf(a[0]), vh = rbf(a[1]); uv[T][0] = pk2(uh, uh); uv[T][1] = pk2(a[0] - uh, vh); uv[T][2] = pk2(vh, a[1] - vh); }
    acc[0] = Z16; acc[1] = Z16; acc[2] = Z16;
    const lp wpp[2] = {opq(wb3), opq(wb3 + 49152)}; u32x4 w[2][3];
    auto ldw = [&](int s) {
#pragma unroll
      for (int c = 0; c < 3; ++c) w[s & 1][c] = fragp(wpp[s >> 4], (s & 15) * 3072 + c * 64, 1536); };
    ST2(21);
    ldw(0); SB0();
#pragma unroll
    for (int s = 0; s < 32; ++s) {
      if (s == 15) bar();
      if (s + D3 < 32) lda(s + D3);
      if (s == 26) { asm volatile("" : "+v"(xa[26]), "+v"(xa[27]), "+v"(xa[28]), "+v"(xa[29]), "+v"(xa[30]), "+v"(xa[31]), "+v"(s0u), "+v"(s1u)); load_wd(6, 12); }
      if (s < 31) ldw(s + 1);
#pragma unroll
      for (int c = 0; c < 3; ++c) acc[c] = mfma(w[s & 1][c], xa[s], acc[c]);
      ilv<3, 2, 0>(); SB0();
    }
    ST(10);
    const f32x4 s0 = __builtin_bit_cast(f32x4, s0u), s1 = __builtin_bit_cast(f32x4, s1u);
    const float mean = hsum((s0[0] + s0[2]) + (s1[0] + s1[2])) * 0.125f;
    const float d0 = s0[0] - mean, d1 = s0[2] - mean, d2 = s1[0] - mean, d3 = s1[2] - mean;
    const float var = hsum((s0[1] + s0[3]) + (s1[1] + s1[3]) + 64.f * (d0 * d0 + d1 * d1 + d2 * d2 + d3 * d3)) * (1.f / 512.f);
    const float rstd = rsqrtf(var + 1e-5f), sd = (var + 1e-5f) * rstd, ah = rbf(-mean), dh = rbf(sd), cg = -1.4426950408889634f * rstd;
    const u32x4 bx = {h ? 0u : pk2(ah, -mean - ah), h ? 0u : pk2(ah, dh), h ? 0u : pk2(sd - dh, dh), 0u};
#pragma unroll
    for (int T = 0; T < 3; ++T) acc[T] = mfma(u32x4{uv[T][0], uv[T][1], uv[T][2], 0u}, bx, acc[T]);
#pragma unroll
    for (int T = 0; T < 3; ++T) { float r[8];
#pragma unroll
      for (int i = 0; i < 8; ++i) r[i] = (acc[T][8 + i] * rstd) * __builtin_amdgcn_rcpf(1.f + __builtin_amdgcn_exp2f(cg * acc[T][i]));
      if (ul + T >= u0 && ul + T < u0 + nu) __builtin_amdgcn_raw_buffer_store_b128(pk8(r), rs, WS_H + (tt * 86 + ul + T) * 1024 + lane * 16, 0, 16);
    }
    ST(29);
    publish(ws, 768 + m3 * 32 + nf, tag); }
  ST(11);
  load_wd(12, 18);
  wait_flags(ws, 768 + (Mt >> 2) * 32, 1, 32, tag, wave, lane);
  ST(12);
  { const unsigned hb = WS_H + ((Mt * 2 + wm2) * 86 + ks4) * 1024;
    auto lda = [&](int i) { xa[i] = ldx(rs, lane * 16, hb + (i == 21 && wq >= 2 ? 20 : i) * 1024); };
#pragma unroll
    for (int i = 0; i < D4; ++i) { lda(i); SB0(); }
    acc[0] = Z16; acc[1] = Z16;
    lp wpp[3][2]; u32x4 w[2][2];
#pragma unroll
    for (int i = 0; i < 6; ++i) wpp[i >> 1][i & 1] = opq((wb8 ^ ((i & 1) * 64)) + (i >> 1) * 49152);
    auto ldw = [&](int i) { const int sl = i < 18 ? i : i - 18; w[i & 1][0] = fragp(wpp[sl / 6][0], (sl % 6) * 8192, 1024); w[i & 1][1] = fragp(wpp[sl / 6][1], (sl % 6) * 8192, 1024); };
    ST2(23);
    ldw(0); SB0();
#pragma unroll
    for (int i = 0; i < 22; ++i) {
      if (i == 3) { bar(); load_wd(18, 22); }
      if (i + D4 < 22) lda(i + D4);
      if (i == 17) { wait_vm0(); bar(); }
      if (i < 21) ldw(i + 1);
      if (i == 21 && wq >= 2) xa[21] = u32x4{0, 0, 0, 0};
      acc[0] = mfma(w[i & 1][0], xa[i], acc[0]); acc[1] = mfma(w[i & 1][1], xa[i], acc[1]);
      ilv<2, 2, 0>(); SB0();
    }
    ST(13);
    float r[8]; reduce4(L0 + 49152, acc, wave, lane, r);
    unsigned o[4];
#pragma unroll
    for (int e = 0; e < 2; ++e) { const f32x4 gv = ldsf4(L0 + L_MOD + (2560 + col2 + 4 * h) * 4 + e * 32);
      o[2 * e] = pk2(lo16(x1p[2 * e]) + gv[0] * r[4 * e], hi16(x1p[2 * e]) + gv[1] * r[4 * e + 1]);
      o[2 * e + 1] = pk2(lo16(x1p[2 * e + 1]) + gv[2] * r[4 * e + 2], hi16(x1p[2 * e + 1]) + gv[3] * r[4 * e + 3]); }
    swap32(o[0], o[2]); swap32(o[1], o[3]);
    *(u32x4*)(out + tok2 * 512 + col2 + 8 * h) = u32x4{o[0], o[1], o[2], o[3]};
  }
  ST(14);
}

extern "C" void kernel_launch(void* const* d_in, const int* in_sizes, int n_in, void* d_out, int out_size, void* d_ws, size_t ws_size,
                              hipStream_t stream) {
  typedef const unsigned short* P;
  adaln_block<<<256, 512, 0, stream>>>((P)d_in[0], (P)d_in[1], (P)d_in[2], (P)d_in[3], (P)d_in[5], (P)d_in[6], (P)d_in[7], (P)d_in[8], (P)d_in[9],
                                       (unsigned short*)d_out, (char*)d_ws);
}
```

```cpp
#include <hip/hip_runtime.h>
#include <hip/hip_bf16.h>
#include <type_traits>

typedef __attribute__((ext_vector_type(8))) __bf16 bf16x8;
typedef __attribute__((ext_vector_type(2))) __bf16 bf16x2;
typedef __attribute__((ext_vector_type(4))) short s16x4;
typedef __attribute__((ext_vector_type(4))) float f32x4;
typedef __attribute__((ext_vector_type(2))) float f32x2;
typedef __attribute__((ext_vector_type(16))) float f32x16;
typedef __attribute__((ext_vector_type(4))) unsigned u32x4;
typedef __attribute__((ext_vector_type(2))) unsigned u32x2;
typedef __attribute__((address_space(1))) unsigned gu32;
typedef __attribute__((address_space(1))) unsigned long long gu64;
#define LAS __attribute__((address_space(3)))
#define RLX_AGENT __ATOMIC_RELAXED, __HIP_MEMORY_SCOPE_AGENT
#define DEV __device__ __forceinline__

constexpr unsigned WS_FLAG = 4096;
constexpr unsigned WS_MOD = WS_FLAG + 4 * 256 * 128;
constexpr unsigned WS_KV = WS_MOD + 16384;
constexpr unsigned WS_ST = WS_KV + 256 * 4352;
constexpr unsigned WS_O = WS_ST + 2048 * 128;
constexpr unsigned WS_X1 = WS_O + 2097152;
constexpr unsigned WS_H = WS_X1 + 2097152;
constexpr unsigned WS_END = WS_H + 128 * 43 * 1024;
constexpr unsigned L_MOD = 147456, L_SCR = 159744;
constexpr int D3 = 10, D4 = 12;
constexpr unsigned SPIN_CAP = 1u << 19;

DEV unsigned pk2(float a, float b) { f32x2 v = {a, b}; return __builtin_bit_cast(unsigned, __builtin_convertvector(v, bf16x2)); }
DEV float lo16(unsigned u) { return __uint_as_float(u << 16); }
DEV float hi16(unsigned u) { return __uint_as_float(u & 0xffff0000u); }
DEV float rbf(float a) { return lo16(pk2(a, a)); }
DEV f32x16 mfma(u32x4 a, u32x4 b, f32x16 c) {
  return __builtin_amdgcn_mfma_f32_32x32x16_bf16(__builtin_bit_cast(bf16x8, a), __builtin_bit_cast(bf16x8, b), c, 0, 0, 0); }
DEV u32x4 pk8(const float* v) { return u32x4{pk2(v[0], v[1]), pk2(v[2], v[3]), pk2(v[4], v[5]), pk2(v[6], v[7])}; }
DEV void swap32(unsigned& a, unsigned& b) { auto s = __builtin_amdgcn_permlane32_swap(a, b, false, false); a = s[0]; b = s[1]; }
DEV float hsum(float x) { unsigned a = __float_as_uint(x), b = a; swap32(a, b); return __uint_as_float(a) + __uint_as_float(b); }
DEV float hmax(float x) { unsigned a = __float_as_uint(x), b = a; swap32(a, b); return fmaxf(__uint_as_float(a), __uint_as_float(b)); }
#define XRED(name, OP) DEV float name(float x) { \
  auto s = __builtin_amdgcn_permlane16_swap(__float_as_uint(x), __float_as_uint(x), false, false); \
  x = OP(__uint_as_float(s[0]), __uint_as_float(s[1])); \
  auto t = __builtin_amdgcn_permlane32_swap(__float_as_uint(x), __float_as_uint(x), false, false); \
  return OP(__uint_as_float(t[0]), __uint_as_float(t[1])); }
DEV float addf(float a, float b) { return a + b; }
XRED(xsum, addf)
template <int N> DEV float ror(float x) { return __int_as_float(__builtin_amdgcn_update_dpp(0, __float_as_int(x), 0x120 + N, 0xf, 0xf, false)); }

DEV void glds(const void* g, unsigned lds) {
  unsigned keep;
  asm volatile("s_mov_b32 %0, m0\n\ts_mov_b32 m0, %2\n\ts_nop 0\n\tglobal_load_lds_dwordx4 %1, off\n\ts_mov_b32 m0, %0"
               : "=&s"(keep) : "v"(g), "s"(lds) : "memory");
}
DEV void touch(unsigned& d, const void* g) { asm volatile("global_load_dword %0, %1, off sc1" : "+v"(d) : "v"(g) : "memory"); }
DEV void keep(unsigned& d) { asm volatile("" : "+v"(d)); }
DEV void wait_vm0() { asm volatile("s_waitcnt vmcnt(0)" ::: "memory"); }
DEV void bar() { asm volatile("s_waitcnt lgkmcnt(0)\n\ts_barrier" ::: "memory"); }
typedef LAS char* lp;
DEV lp opq(unsigned a) { asm volatile("" : "+v"(a)); return (lp)a; }
DEV u32x4 fragp(lp a, int o, int d) {
  u32x2 x = __builtin_bit_cast(u32x2, __builtin_amdgcn_ds_read_tr16_b64_v4i16((LAS s16x4*)(a + o)));
  u32x2 y = __builtin_bit_cast(u32x2, __builtin_amdgcn_ds_read_tr16_b64_v4i16((LAS s16x4*)(a + o + d)));
  return u32x4{x[0], x[1], y[0], y[1]}; }
DEV f32x4 ldp4(lp a, int o) { return *(LAS f32x4*)(a + o); }
template <int I, int N, class F> DEV void sfor(F f) { if constexpr (I < N) { f(std::integral_constant<int, I>{}); sfor<I + 1, N>(f); } }
#define SB0() __builtin_amdgcn_sched_barrier(0)
template <int NM, int ND, int NV> DEV void ilv() {
#pragma unroll
  for (int i = 0; i < NM; ++i) { __builtin_amdgcn_sched_group_barrier(0x008, 1, 0); __builtin_amdgcn_sched_group_barrier(0x100, ND, 0);
    if (NV) __builtin_amdgcn_sched_group_barrier(0x002, NV, 0); }
}
DEV f32x4 ldsf4(unsigned a) { return *(LAS f32x4*)a; }
DEV void stsf4(unsigned a, f32x4 v) { *(LAS f32x4*)a = v; }

DEV void wait_flags(char* ws, unsigned first, unsigned stride, unsigned n, unsigned tag, int wave, int lane) {
  if (wave == 0) {
    gu32* p = (gu32*)(ws + WS_FLAG) + (first + (lane & (n - 1)) * stride) * 32;
#pragma unroll 1
    for (unsigned s = 0; s < SPIN_CAP; ++s) {
      if (__all(__hip_atomic_load(p, RLX_AGENT) == tag)) break;
      __builtin_amdgcn_s_sleep(1);
    }
  }
  bar();
}
DEV void publish(char* ws, unsigned idx, unsigned tag) {
  wait_vm0(); bar();
  if (threadIdx.x == 0) __hip_atomic_store((gu32*)(ws + WS_FLAG) + idx * 32, tag, RLX_AGENT);
}
DEV u32x4 ldx(__amdgpu_buffer_rsrc_t rs, unsigned v, unsigned s) { return __builtin_amdgcn_raw_buffer_load_b128(rs, v, s, 16); }
#define ST(i)
#define ST2(i)

DEV void hedgehog(u32x4 wp, const float* v, u32x4& o0, u32x4& o1) {
  const float L = 1.4426950408889634f; f32x16 Z = {0}; f32x16 pp = mfma(wp, pk8(v), Z); float e[16];
#pragma unroll
  for (int s = 0; s < 2; ++s) {
    float m = fmaxf(fmaxf(fmaxf(pp[8 * s], pp[8 * s + 1]), fmaxf(pp[8 * s + 2], pp[8 * s + 3])), fmaxf(fmaxf(pp[8 * s + 4], pp[8 * s + 5]), fmaxf(pp[8 * s + 6], pp[8 * s + 7])));
    m = -hmax(m) * L; float t = 0;
#pragma unroll
    for (int i = 0; i < 8; ++i) { e[8 * s + i] = __builtin_amdgcn_exp2f(pp[8 * s + i] * L + m); t += e[8 * s + i]; }
    t = __builtin_amdgcn_rcpf(hsum(t));
#pragma unroll
    for (int i = 0; i < 8; ++i) e[8 * s + i] *= t;
  }
  o0 = pk8(e); o1 = pk8(e + 8);
}
DEV void reduce4(unsigned base, const f32x16 (&acc)[3], int wave, int lane, float* r) {
  const int wm = wave & 1, wq = wave >> 1;
#pragma unroll
  for (int d = 0; d < 4; ++d) {
    const f32x16 v = acc[d >> 1]; const int o = (d & 1) * 8;
    if (d != wq) { const unsigned a = base + ((d * 2 + wm) * 3 + ((wq - d - 1) & 3)) * 2048 + lane * 16;
      stsf4(a, f32x4{v[o], v[o + 1], v[o + 2], v[o + 3]}); stsf4(a + 1024, f32x4{v[o + 4], v[o + 5], v[o + 6], v[o + 7]}); }
    else {
#pragma unroll
      for (int i = 0; i < 8; ++i) r[i] = v[o + i]; }
  }
  bar();
#pragma unroll
  for (int s = 0; s < 6; ++s) { const f32x4 v = ldsf4(base + wave * 6144 + s * 1024 + lane * 16);
#pragma unroll
    for (int i = 0; i < 4; ++i) r[(s & 1) * 4 + i] += v[i]; }
}

__global__ void __launch_bounds__(512, 2)
adaln_block(const unsigned short* __restrict__ x, const unsigned short* __restrict__ wqkv, const unsigned short* __restrict__ wphi,
            const unsigned short* __restrict__ wo, const unsigned short* __restrict__ wg, const unsigned short* __restrict__ wu,
            const unsigned short* __restrict__ wd, const unsigned short* __restrict__ cin, const unsigned short* __restrict__ wmod,
            unsigned short* __restrict__ out, char* ws) {
  __shared__ __attribute__((aligned(16))) char smem[163840];
  const unsigned L0 = (unsigned)(unsigned long long)(LAS char*)smem;
  const int tid = threadIdx.x, lane = tid & 63, wave = __builtin_amdgcn_readfirstlane(tid >> 6);
  const int h = lane >> 5, r32 = lane & 31, cb = (lane >> 4) & 1, q = (lane & 15) >> 2, p = lane & 3;
  const int b = blockIdx.x, xc = b & 7, j = b >> 3;
  const int mt = (xc >> 1) * 4 + (j & 3), nt = (xc & 1) * 8 + (j >> 2);
  const int Mt = xc * 4 + (j & 3), Nt = j >> 2;
  const int m3 = (xc >> 1) * 2 + (j & 1), nf = (xc & 1) * 16 + (j >> 1);
  const int wm = wave & 3, wk = wave >> 2, wm2 = wave & 1, wq = wave >> 1;
  const __amdgpu_buffer_rsrc_t rs = __builtin_amdgcn_make_buffer_rsrc(ws, 0, WS_END, 0x00020000);
  const unsigned wbase = L0 + wave * 1024;
  const f32x16 Z16 = {0};
  ST(0);
  unsigned tagv, cv[4]; u32x4 wv[4]; const int gb = xc * 12 + j;
  asm volatile("global_load_dword %0, %1, off sc1" : "=v"(tagv) : "v"(ws) : "memory");
  if (j < 12) {
#pragma unroll
    for (int i = 0; i < 4; ++i) { const int row = (tid >> 2) + 128 * i;
      asm volatile("global_load_dwordx4 %0, %2, off\n\tglobal_load_ushort %1, %3, off" : "=&v"(wv[i]), "=&v"(cv[i]) : "v"(wmod + row * 3072 + gb * 32 + p * 8), "v"(cin + row) : "memory"); } }
  unsigned pf = 0;
  touch(pf, x + (xc >> 1) * 262144 + (j * 128 + wave * 16 + (lane & 15)) * 64);
#pragma unroll
  for (int n = 0; n < 16; ++n) glds(x + (mt * 128 + wm * 32 + 2 * n + h) * 512 + wk * 256 + ((r32 ^ ((2 * n + h) & 15)) * 8), L0 + wave * 16384 + n * 1024);
  { const int l = r32 < 24 ? r32 : 0, r = l / 12, c = l - r * 12; touch(pf, wqkv + (j * 16 + wave * 2 + r) * 1536 + (c >> 2) * 512 + ((xc & 1) * 4 + (c & 3)) * 64); }
  asm volatile("s_waitcnt vmcnt(17)" : "+v"(tagv), "+v"(wv[0]), "+v"(wv[1]), "+v"(wv[2]), "+v"(wv[3]), "+v"(cv[0]), "+v"(cv[1]), "+v"(cv[2]), "+v"(cv[3]) :: "memory");
  u32x4 xa[32];
  const int u0 = nf < 22 ? 3 * nf : 2 * nf + 22, nu = nf < 22 ? 3 : 2, ul = u0 > 83 ? 83 : u0;
  const unsigned short *gsrc[3], *qsrc[3];
#pragma unroll
  for (int i = 0; i < 3; ++i) {
    const int c0 = i * 512 + tid, row = c0 / 12, cc = c0 - row * 12;
    qsrc[i] = wqkv + row * 1536 + (cc >> 2) * 512 + nt * 32 + (cc & 3) * 8;
  }
  auto load_gu = [&](int n0, int n1) {
#pragma unroll
    for (int n = n0; n < n1; ++n) glds(gsrc[n % 3] + (n / 3) * 128 * 1376, wbase + 49152 + n * 8192); };
  const int off8 = (wm2 * 8 + (lane >> 3)) * 512 + Nt * 64 + ((((lane >> 2) ^ (lane >> 4)) & 1) * 32) + p * 8;
  const int ks4 = wq * 22 - (wq == 3);
  auto load_wd = [&](int i0, int i1) {
#pragma unroll
    for (int i = i0; i < i1; ++i) glds(wd + off8 + (i == 21 && wq == 3 ? 85 : ks4 + i) * 8192, wbase + (i < 18 ? i : i - 18) * 8192); };

  auto load_wo = [&](int i0, int i1) {
#pragma unroll
    for (int i = i0; i < i1; ++i) glds(wo + off8 + (wq * 8 + i) * 8192, wbase + (i < 6 ? 98304 : 0) + i * 8192); };
  const unsigned tag = __builtin_amdgcn_readfirstlane(tagv) + 1u;
  if (j < 12) {
    f32x2 a[4] = {{0, 0}, {0, 0}, {0, 0}, {0, 0}};
#pragma unroll
    for (int i = 0; i < 4; ++i) {
      float c = lo16(cv[i]); c = rbf(c * __builtin_amdgcn_rcpf(1.f + __expf(-c)));
#pragma unroll
      for (int e = 0; e < 4; ++e) a[e] += c * f32x2{lo16(wv[i][e]), hi16(wv[i][e])};
    }
#pragma unroll
    for (int e = 0; e < 8; ++e) { float v = a[e >> 1][e & 1]; v += ror<4>(v); v += ror<8>(v); a[e >> 1][e & 1] = v; }
    if (q == 0) { const unsigned d = L0 + L_MOD + (tid >> 4) * 128 + p * 32;
      stsf4(d, f32x4{a[0][0], a[0][1], a[1][0], a[1][1]}); stsf4(d + 16, f32x4{a[2][0], a[2][1], a[3][0], a[3][1]}); }
    bar();
    if (wave == 0) { f32x2 s = {0, 0};
#pragma unroll
      for (int w = 0; w < 8; ++w) s += *(LAS f32x2*)(L0 + L_MOD + w * 512 + lane * 8);
      s[0] = xsum(s[0]); s[1] = xsum(s[1]);
      if (lane < 16) __hip_atomic_store((gu64*)(ws + WS_MOD) + gb * 16 + lane, ((unsigned long long)tag << 32) | pk2(s[0], s[1]), RLX_AGENT); } }
  asm volatile("s_waitcnt vmcnt(1)" ::: "memory");
  { const unsigned xb = L0 + wave * 16384 + r32 * 512 + ((h ^ (r32 & 15)) * 16);
#pragma unroll
    for (int st = 0; st < 16; ++st) xa[st] = *(LAS u32x4*)(xb ^ (st * 32)); }
  bar();
  ST(15);
  f32x2 t[64]; u32x4 g[4];
  auto ldg = [&]() {
#pragma unroll
    for (int i = 0; i < 4; ++i) g[i] = ldx(rs, WS_MOD + (wk * 256 + (lane & 15) * 16 + h * 8) * 4 + (i >> 1) * 2048 + (i & 1) * 16, 0); };
  { f32x2 sv = {0, 0}, vv = {0, 0};
#pragma unroll
    for (int s = 0; s < 4; ++s) {
#pragma unroll
      for (int i = 0; i < 3; ++i) glds(qsrc[i] + s * 128 * 1536, wbase + (s * 3 + i) * 8192);
      SB0();
#pragma unroll
      for (int i = 0; i < 16; ++i) { t[16 * s + i] = f32x2{lo16(xa[4 * s + (i >> 2)][i & 3]), hi16(xa[4 * s + (i >> 2)][i & 3])}; sv += t[16 * s + i]; vv += t[16 * s + i] * t[16 * s + i]; }
      SB0(); }
    ST(16);
    const float sh = hsum(sv[0] + sv[1]), vh = hsum(vv[0] + vv[1]);
    if (h == 0) *(LAS f32x2*)(L0 + L_SCR + 1024 + wave * 256 + r32 * 8) = f32x2{sh, vh};
    bar();
    ldg(); SB0();
    const f32x2 o = *(LAS f32x2*)(L0 + L_SCR + 1024 + (wave ^ 4) * 256 + r32 * 8);
    const float mean = (sh + o[0]) * (1.f / 512.f), rstd = rsqrtf(fmaxf((vh + o[1]) * (1.f / 512.f) - mean * mean, 0.f) + 1e-5f), dl = -mean * rstd;
#pragma unroll
    for (int i = 0; i < 64; ++i) { t[i] = t[i] * rstd + dl; asm volatile("" : "+v"(t[i])); } }
  ST(1);
  u32x4 wp;
  { const unsigned short* pp = wphi + (nt * 2 + wk) * 256 + 4 * h * 16 + (lane & 15); const unsigned sg = cb ? 0x80008000u : 0u;
#pragma unroll
    for (int e = 0; e < 4; ++e) { const int d = (e >> 1) * 8 + (e & 1) * 2; wp[e] = ((unsigned)pp[d * 16] | ((unsigned)pp[d * 16 + 16] << 16)) ^ sg; } }
  float ms[8], mc[8];
  {
#pragma unroll 1
    for (unsigned s = 0; s < SPIN_CAP; ++s) { bool ok = true;
#pragma unroll
      for (int i = 0; i < 4; ++i) ok = ok && g[i][1] == tag && g[i][3] == tag;
      if (__all(ok)) break;
      asm volatile("s_sleep 1" ::: "memory");
      ldg();
    }
#pragma unroll
    for (int i = 0; i < 4; ++i) { const unsigned u = g[i >> 1][(i & 1) * 2], v = g[2 + (i >> 1)][(i & 1) * 2], c = pk2(1.f + lo16(v), 1.f + hi16(v));
      ms[2 * i] = lo16(u); ms[2 * i + 1] = hi16(u); mc[2 * i] = lo16(c); mc[2 * i + 1] = hi16(c); } }
  asm volatile("s_waitcnt vmcnt(0)" : "+v"(wp), "+v"(pf) :: "memory"); bar();
  ST(2);
  { const int l = (wave - 2) * 64 + lane, sg = l / 12, o = (j * 32 + sg) >> 1;
    const int e = o * 1376 + (xc & 1) * 768 + (l - sg * 12) * 64;
    touch(pf, wave < 2 ? wo + (j * 128 + wave * 64 + lane) * 64 : ((sg & 1) ? wu : wg) + (e < 704510 ? e : 704510)); }
  f32x16 acc[3] = {Z16, Z16, Z16};
  { const lp wb = opq(L0 + (wk * 256 + h * 8 + q) * 192 + cb * 32 + p * 8);
    u32x4 w[2][3], hh[2];
    auto ldw = [&](int st, int c) { w[st & 1][c] = fragp(wb, st * 3072 + c * 64, 768); };
    auto mkh = [&](auto S, int e) {
      constexpr int st = S; float v0 = t[st * 4 + e][0], v1 = t[st * 4 + e][1];
      asm("v_mul_f32_dpp %0, %2, %0 row_newbcast:%6 row_mask:0xf bank_mask:0xf\n\tv_mul_f32_dpp %1, %3, %1 row_newbcast:%6 row_mask:0xf bank_mask:0xf\n\t"
          "v_add_f32_dpp %0, %4, %0 row_newbcast:%6 row_mask:0xf bank_mask:0xf\n\tv_add_f32_dpp %1, %5, %1 row_newbcast:%6 row_mask:0xf bank_mask:0xf"
          : "+v"(v0), "+v"(v1) : "v"(mc[2 * e]), "v"(mc[2 * e + 1]), "v"(ms[2 * e]), "v"(ms[2 * e + 1]), "i"(st));
      hh[st & 1][e] = pk2(v0, v1); };
#pragma unroll
    for (int e = 0; e < 8; ++e) asm volatile("" : "+v"(ms[e]), "+v"(mc[e]));
    asm volatile("s_nop 1"); SB0();
    ldw(0, 0); ldw(0, 1); ldw(0, 2);
#pragma unroll
    for (int e = 0; e < 4; ++e) mkh(std::integral_constant<int, 0>{}, e);
    SB0();
    sfor<0, 16>([&](auto S) { constexpr int st = S; std::integral_constant<int, (st + 1) & 15> N;
      acc[0] = mfma(w[st & 1][0], hh[st & 1], acc[0]); SB0();
      if (st < 15) { ldw(st + 1, 0); mkh(N, 0); mkh(N, 1); }
      SB0(); acc[1] = mfma(w[st & 1][1], hh[st & 1], acc[1]); SB0();
      if (st < 15) { ldw(st + 1, 1); mkh(N, 2); }
      SB0(); acc[2] = mfma(w[st & 1][2], hh[st & 1], acc[2]); SB0();
      if (st < 15) { ldw(st + 1, 2); mkh(N, 3); }
      if (st >= 5 && st < 11) load_wo(st - 5, st - 4);
      SB0(); }); }
  ST(3);
  u32x4 phq0, phq1, gm;
  { float v[3][8];
    bar(); load_wo(6, 8);
    auto send = [&](int o) {
#pragma unroll
      for (int s = 0; s < 6; ++s) { const f32x16 a = acc[s >> 1]; const int i = o + (s & 1) * 4; stsf4(L0 + wave * 6144 + s * 1024 + lane * 16, f32x4{a[i], a[i + 1], a[i + 2], a[i + 3]}); } };
    auto recv = [&](int o) {
#pragma unroll
      for (int s = 0; s < 6; ++s) { const f32x4 r = ldsf4(L0 + (wave ^ 4) * 6144 + s * 1024 + lane * 16);
#pragma unroll
        for (int i = 0; i < 4; ++i) v[s >> 1][(s & 1) * 4 + i] = acc[s >> 1][o + (s & 1) * 4 + i] + r[i]; } };
    if (wk) { send(0); bar(); recv(8); } else { send(8); bar(); recv(0); }
    ST(17);
    u32x4 k0, k1; hedgehog(wp, v[1], k0, k1);
    const u32x4 vb = pk8(v[2]);
    const unsigned img = L0 + 65536 + wave * 4096, wr = img + r32 * 104 + h * 8;
    *(LAS u32x2*)(wr) = u32x2{k0[0], k0[1]}; *(LAS u32x2*)(wr + 16) = u32x2{k0[2], k0[3]}; *(LAS u32x2*)(wr + 32) = u32x2{k1[0], k1[1]}; *(LAS u32x2*)(wr + 48) = u32x2{k1[2], k1[3]};
    *(LAS u32x2*)(wr + 64) = u32x2{vb[0], vb[1]}; *(LAS u32x2*)(wr + 80) = u32x2{vb[2], vb[3]};
    asm volatile("" ::: "memory");
    f32x16 kv = Z16; const lp ra = opq(img + (8 * h + q) * 104 + cb * 32 + p * 8), rb = opq(img + (8 * h + q) * 104 + 64 + p * 8);
#pragma unroll
    for (int s = 0; s < 2; ++s) { u32x4 bb = fragp(rb, s * 1664, 416);
#pragma unroll
      for (int e = 0; e < 4; ++e) bb[e] = cb ? 0x3f803f80u : bb[e];
      kv = mfma(fragp(ra, s * 1664, 416), bb, kv); }
    asm volatile("" ::: "memory");
    ST(18);
    if (r32 <= 16) {
#pragma unroll
      for (int c = 0; c < 4; ++c) stsf4(img + (c * 34 + h * 17 + r32) * 16, f32x4{kv[4 * c], kv[4 * c + 1], kv[4 * c + 2], kv[4 * c + 3]}); }
    bar();
    if (tid < 136) { const int c = tid / 34; const unsigned a = L0 + 65536 + (c >> 1) * 16384 + ((c & 1) * 68 + tid - c * 34) * 16;
      const f32x4 s = (ldsf4(a) + ldsf4(a + 4096)) + (ldsf4(a + 8192) + ldsf4(a + 12288)), r = (ldsf4(a + 544) + ldsf4(a + 4640)) + (ldsf4(a + 8736) + ldsf4(a + 12832));
      __builtin_amdgcn_raw_buffer_store_b128(u32x4{pk2(s[0], s[1]), pk2(s[2], s[3]), pk2(r[0], r[1]), pk2(r[2], r[3])}, rs, WS_KV + (nt * 16 + mt) * 2176 + tid * 16, 0, 16); }
    ST(20);
    publish(ws, mt * 16 + nt, tag); keep(pf);
    ST(4);
    gm = ldx(rs, WS_MOD + 4096 + tid * 16, 0);
    int tid2 = tid; asm volatile("" : "+v"(tid2));
#pragma unroll
    for (int i = 0; i < 3; ++i) { const int c0 = i * 512 + tid2, row = c0 / 12, cc = c0 - row * 12;
      gsrc[i] = ((cc & 2) ? wu : wg) + row * 1376 + (ul + (cc >> 2)) * 16 + (cc & 1) * 8; }
    hedgehog(wp, v[0], phq0, phq1); }
  const int tok2 = Mt * 64 + wm2 * 32 + r32, col2 = Nt * 64 + wq * 16;
  const u32x4 xr = *(const u32x4*)(x + tok2 * 512 + col2 + 8 * h);
  const unsigned wb8 = L0 + wq * 2048 + (4 * h + q) * 128 + cb * 32 + p * 8 + (q >> 1) * 64;
  u32x4 wf[8][2];
  { const lp wa[4] = {opq(wb8 + 98304), opq((wb8 ^ 64) + 98304), opq(wb8), opq(wb8 ^ 64)};
#pragma unroll
    for (int i = 0; i < 16; ++i) wf[i >> 1][i & 1] = fragp(wa[(i & 1) + (i < 12 ? 0 : 2)], (i >> 1) * 8192, 1024); }
#pragma unroll 1
  for (unsigned s = 0; s < SPIN_CAP; ++s) {
    if (__all(gm[1] == tag && gm[3] == tag)) break;
    asm volatile("s_sleep 1" ::: "memory");
    gm = ldx(rs, WS_MOD + 4096 + tid * 16, 0);
  }
  if (wq == 2) { gm[0] = pk2(1.f + lo16(gm[0]), 1.f + hi16(gm[0])); gm[2] = pk2(1.f + lo16(gm[2]), 1.f + hi16(gm[2])); }
  stsf4(L0 + L_MOD + 4096 + tid * 16, f32x4{lo16(gm[0]), hi16(gm[0]), lo16(gm[2]), hi16(gm[2])});
  if (wq == 1 || wq == 2) *(LAS u32x2*)(L0 + L_MOD + (2 - wq) * 1024 + (tid & 124) * 8 + (tid & 1) * 16 + (tid & 2) * 4) = u32x2{gm[0], gm[2]};
  load_gu(2, 6);
  wait_flags(ws, nt, 16, 16, tag, wave, lane);
  ST(5);
  { if (tid < 272) { u32x4 v[8]; const int hf = tid >= 136;
#pragma unroll
      for (int i = 0; i < 8; ++i) v[i] = ldx(rs, WS_KV + (nt * 16 + hf * 7) * 2176 + tid * 16, i * 2176);
      f32x2 s[4] = {{0, 0}, {0, 0}, {0, 0}, {0, 0}};
#pragma unroll
      for (int i = 0; i < 32; ++i) s[i & 3] += f32x2{lo16(v[i >> 2][i & 3]), hi16(v[i >> 2][i & 3])};
      stsf4(L0 + tid * 16, f32x4{s[0][0], s[0][1], s[1][0], s[1][1]}); stsf4(L0 + 4352 + tid * 16, f32x4{s[2][0], s[2][1], s[3][0], s[3][1]}); }
    bar();
    ST(22);
    const unsigned a = L0 + (wk * 68 + h * 17 + (r32 < 16 ? r32 : 16)) * 16; u32x4 ka[2];
#pragma unroll
    for (int i = 0; i < 4; ++i) { const unsigned o = a + (i >> 1) * 544 + (i & 1) * 4352; const f32x4 k = ldsf4(o) + ldsf4(o + 2176);
      ka[i >> 1][(i & 1) * 2] = pk2(k[0], k[1]); ka[i >> 1][(i & 1) * 2 + 1] = pk2(k[2], k[3]); }
    const f32x16 d = mfma(ka[1], phq1, mfma(ka[0], phq0, Z16));
    const float rd = __builtin_amdgcn_rcpf(d[8] + 1e-6f); float o[8];
#pragma unroll
    for (int i = 0; i < 8; ++i) o[i] = d[i] * rd;
    __builtin_amdgcn_raw_buffer_store_b128(pk8(o), rs, WS_O + ((mt * 4 + wm) * 32 + nt * 2 + wk) * 1024 + lane * 16, 0, 16);
    ST(24);
    publish(ws, 256 + mt * 16 + nt, tag); }
  ST(6);
  load_gu(0, 2); load_gu(6, 9);
  if (wave >= 2 && tid < 472) touch(pf, wd + (j * 344 + tid - 128) * 64);
  wait_flags(ws, 256 + (Mt >> 1) * 16, 1, 16, tag, wave, lane);
  if (b == 0 && tid == 0) __hip_atomic_store((gu32*)ws, tag, RLX_AGENT);
  ST(7);
  if (wave == 0) touch(pf, ws + WS_O + Mt * 65536 + (Nt * 64 + lane) * 128);
  u32x4 x1p;
  {
#pragma unroll
    for (int i = 0; i < 8; ++i) { xa[i] = ldx(rs, lane * 16, WS_O + ((Mt * 2 + wm2) * 32 + wq * 8 + i) * 1024); SB0(); }
    acc[0] = Z16; acc[1] = Z16;
#pragma unroll
    for (int i = 0; i < 8; ++i) { acc[0] = mfma(wf[i][0], xa[i], acc[0]); acc[1] = mfma(wf[i][1], xa[i], acc[1]); SB0(); }
    load_gu(9, 12);
    ST(25);
    float r[8]; reduce4(L0, acc, wave, lane, r);
    unsigned xs[4] = {xr[0], xr[1], xr[2], xr[3]}; swap32(xs[0], xs[2]); swap32(xs[1], xs[3]);
    const unsigned ma = L0 + L_MOD + (col2 + 4 * h) * 4; float y[8], s = 0, v = 0;
#pragma unroll
    for (int e = 0; e < 2; ++e) { const f32x4 ga = ldsf4(ma + 4096 + e * 32), ca = ldsf4(ma + 8192 + e * 32);
      x1p[2 * e] = pk2(lo16(xs[2 * e]) + ga[0] * r[4 * e], hi16(xs[2 * e]) + ga[1] * r[4 * e + 1]);
      x1p[2 * e + 1] = pk2(lo16(xs[2 * e + 1]) + ga[2] * r[4 * e + 2], hi16(xs[2 * e + 1]) + ga[3] * r[4 * e + 3]);
#pragma unroll
      for (int i = 0; i < 4; ++i) { r[4 * e + i] = (i & 1) ? hi16(x1p[2 * e + (i >> 1)]) : lo16(x1p[2 * e + (i >> 1)]); y[4 * e + i] = r[4 * e + i] * ca[i]; s += r[4 * e + i]; } }
    __builtin_amdgcn_raw_buffer_store_b128(pk8(y), rs, WS_X1 + ((Mt * 2 + wm2) * 32 + Nt * 4 + wq) * 1024 + lane * 16, 0, 16);
    s = hsum(s) * (1.f / 16.f);
#pragma unroll
    for (int i = 0; i < 8; ++i) v += (r[i] - s) * (r[i] - s);
    v = hsum(v);
    if (h == 0) *(LAS f32x2*)(L0 + L_SCR + wave * 256 + r32 * 8) = f32x2{s, v};
    bar();
    if (tid < 64) { f32x2 a[4];
#pragma unroll
      for (int i = 0; i < 4; ++i) a[i] = *(LAS f32x2*)(L0 + L_SCR + i * 512 + tid * 8);
      const float m = ((a[0][0] + a[1][0]) + (a[2][0] + a[3][0])) * 0.25f; float M = 0;
#pragma unroll
      for (int i = 0; i < 4; ++i) M += a[i][1] + 16.f * (a[i][0] - m) * (a[i][0] - m);
      __builtin_amdgcn_raw_buffer_store_b64(u32x2{__float_as_uint(m), __float_as_uint(M)}, rs, WS_ST + (((Mt * 2 + h) * 4 + (Nt >> 1)) * 32 + r32) * 16 + (Nt & 1) * 8, 0, 16); }
    ST(26);
    publish(ws, 512 + Mt * 8 + Nt, tag); keep(pf); }
  ST(8);
  load_wd(0, 6);
  const unsigned wb3 = L0 + 49152 + (4 * h + q) * 192 + cb * 32 + p * 8;
  if (wave >= 2) { f32x16 d = Z16; u32x4 fa[16], fb[16];
    const lp wpt = opq(wb3 + (wq - 1) * 64 + wm2 * 49152), mp = opq(L0 + L_MOD + (r32 == 1) * 1024 + wm2 * 512 + h * 16);
#pragma unroll
    for (int s = 0; s < 16; ++s) { fa[s] = *(LAS u32x4*)(mp + s * 32); fb[s] = fragp(wpt, s * 3072, 1536); }
    SB0();
#pragma unroll
    for (int s = 0; s < 16; ++s) d = mfma(fa[s], fb[s], d);
    if (h == 0) *(LAS f32x2*)(L0 + L_SCR + (wave - 2) * 256 + r32 * 8) = f32x2{d[0], d[1]}; }
  ST(28);
  wait_flags(ws, 512 + m3 * 32, 1, 32, tag, wave, lane);
  ST(9);
  if (wave < 2) touch(pf, ws + WS_X1 + m3 * 262144 + ((j >> 1) * 128 + tid) * 128);
  { const int tt = m3 * 8 + wave;
    auto lda = [&](int s) { xa[s] = ldx(rs, lane * 16, WS_X1 + (tt * 32 + s) * 1024); };
#pragma unroll
    for (int s = 0; s < D3; ++s) { lda(s); SB0(); }
    u32x4 s0u = ldx(rs, WS_ST + ((tt * 4 + 2 * h) * 32 + r32) * 16, 0), s1u = ldx(rs, WS_ST + ((tt * 4 + 2 * h + 1) * 32 + r32) * 16, 0);
    unsigned uv[3][3];
#pragma unroll
    for (int T = 0; T < 3; ++T) { const f32x2 a = *(LAS f32x2*)(L0 + L_SCR + T * 512 + r32 * 8) + *(LAS f32x2*)(L0 + L_SCR + T * 512 + 256 + r32 * 8);
      const float uh = rbf(a[0]), vh = rbf(a[1]); uv[T][0] = pk2(uh, uh); uv[T][1] = pk2(a[0] - uh, vh); uv[T][2] = pk2(vh, a[1] - vh); }
    acc[0] = Z16; acc[1] = Z16; acc[2] = Z16;
    const lp wpp[2] = {opq(wb3), opq(wb3 + 49152)}; u32x4 w[2][3];
    auto ldw = [&](int s) {
#pragma unroll
      for (int c = 0; c < 3; ++c) w[s & 1][c] = fragp(wpp[s >> 4], (s & 15) * 3072 + c * 64, 1536); };
    ST2(21);
    ldw(0); SB0();
#pragma unroll
    for (int s = 0; s < 32; ++s) {
      if (s == 15) bar();
      if (s + D3 < 32) lda(s + D3);
      if (s == 26) { asm volatile("" : "+v"(xa[26]), "+v"(xa[27]), "+v"(xa[28]), "+v"(xa[29]), "+v"(xa[30]), "+v"(xa[31]), "+v"(s0u), "+v"(s1u)); load_wd(6, 12); }
      if (s < 31) ldw(s + 1);
#pragma unroll
      for (int c = 0; c < 3; ++c) acc[c] = mfma(w[s & 1][c], xa[s], acc[c]);
      ilv<3, 2, 0>(); SB0();
    }
    ST(10);
    const f32x4 s0 = __builtin_bit_cast(f32x4, s0u), s1 = __builtin_bit_cast(f32x4, s1u);
    const float mean = hsum((s0[0] + s0[2]) + (s1[0] + s1[2])) * 0.125f;
    const float d0 = s0[0] - mean, d1 = s0[2] - mean, d2 = s1[0] - mean, d3 = s1[2] - mean;
    const float var = hsum((s0[1] + s0[3]) + (s1[1] + s1[3]) + 64.f * (d0 * d0 + d1 * d1 + d2 * d2 + d3 * d3)) * (1.f / 512.f);
    const float rstd = rsqrtf(var + 1e-5f), sd = (var + 1e-5f) * rstd, ah = rbf(-mean), dh = rbf(sd), cg = -1.4426950408889634f * rstd;
    const u32x4 bx = {h ? 0u : pk2(ah, -mean - ah), h ? 0u : pk2(ah, dh), h ? 0u : pk2(sd - dh, dh), 0u};
#pragma unroll
    for (int T = 0; T < 3; ++T) acc[T] = mfma(u32x4{uv[T][0], uv[T][1], uv[T][2], 0u}, bx, acc[T]);
#pragma unroll
    for (int T = 0; T < 3; ++T) { float r[8];
#pragma unroll
      for (int i = 0; i < 8; ++i) r[i] = (acc[T][8 + i] * rstd) * __builtin_amdgcn_rcpf(1.f + __builtin_amdgcn_exp2f(cg * acc[T][i]));
      if (ul + T >= u0 && ul + T < u0 + nu) __builtin_amdgcn_raw_buffer_store_b128(pk8(r), rs, WS_H + (tt * 86 + ul + T) * 1024 + lane * 16, 0, 16);
    }
    ST(29);
    publish(ws, 768 + m3 * 32 + nf, tag); keep(pf); }
  ST(11);
  load_wd(12, 18);
  wait_flags(ws, 768 + (Mt >> 2) * 32, 1, 32, tag, wave, lane);
  ST(12);
  if (tid < 172) touch(pf, ws + WS_H + Mt * 176128 + (Nt * 172 + tid) * 128);
  { const unsigned hb = WS_H + ((Mt * 2 + wm2) * 86 + ks4) * 1024;
    auto lda = [&](int i) { xa[i] = ldx(rs, lane * 16, hb + (i == 21 && wq >= 2 ? 20 : i) * 1024); };
#pragma unroll
    for (int i = 0; i < D4; ++i) { lda(i); SB0(); }
    acc[0] = Z16; acc[1] = Z16;
    lp wpp[3][2]; u32x4 w[2][2];
#pragma unroll
    for (int i = 0; i < 6; ++i) wpp[i >> 1][i & 1] = opq((wb8 ^ ((i & 1) * 64)) + (i >> 1) * 49152);
    auto ldw = [&](int i) { const int sl = i < 18 ? i : i - 18; w[i & 1][0] = fragp(wpp[sl / 6][0], (sl % 6) * 8192, 1024); w[i & 1][1] = fragp(wpp[sl / 6][1], (sl % 6) * 8192, 1024); };
    ST2(23);
    ldw(0); SB0();
#pragma unroll
    for (int i = 0; i < 22; ++i) {
      if (i == 3) { bar(); load_wd(18, 22); }
      if (i + D4 < 22) lda(i + D4);
      if (i == 17) { wait_vm0(); keep(pf); bar(); }
      if (i < 21) ldw(i + 1);
      if (i == 21 && wq >= 2) xa[21] = u32x4{0, 0, 0, 0};
      acc[0] = mfma(w[i & 1][0], xa[i], acc[0]); acc[1] = mfma(w[i & 1][1], xa[i], acc[1]);
      ilv<2, 2, 0>(); SB0();
    }
    ST(13);
    float r[8]; reduce4(L0 + 49152, acc, wave, lane, r);
    unsigned o[4];
#pragma unroll
    for (int e = 0; e < 2; ++e) { const f32x4 gv = ldsf4(L0 + L_MOD + (2560 + col2 + 4 * h) * 4 + e * 32);
      o[2 * e] = pk2(lo16(x1p[2 * e]) + gv[0] * r[4 * e], hi16(x1p[2 * e]) + gv[1] * r[4 * e + 1]);
      o[2 * e + 1] = pk2(lo16(x1p[2 * e + 1]) + gv[2] * r[4 * e + 2], hi16(x1p[2 * e + 1]) + gv[3] * r[4 * e + 3]); }
    swap32(o[0], o[2]); swap32(o[1], o[3]);
    *(u32x4*)(out + tok2 * 512 + col2 + 8 * h) = u32x4{o[0], o[1], o[2], o[3]};
  }
  ST(14);
}

extern "C" void kernel_launch(void* const* d_in, const int* in_sizes, int n_in, void* d_out, int out_size, void* d_ws, size_t ws_size,
                              hipStream_t stream) {
  typedef const unsigned short* P;
  adaln_block<<<256, 512, 0, stream>>>((P)d_in[0], (P)d_in[1], (P)d_in[2], (P)d_in[3], (P)d_in[5], (P)d_in[6], (P)d_in[7], (P)d_in[8], (P)d_in[9],
                                       (unsigned short*)d_out, (char*)d_ws);
}
```

```cpp
#include <hip/hip_runtime.h>
#include <hip/hip_bf16.h>
#include <type_traits>

typedef __attribute__((ext_vector_type(8))) __bf16 bf16x8;
typedef __attribute__((ext_vector_type(2))) __bf16 bf16x2;
typedef __attribute__((ext_vector_type(4))) short s16x4;
typedef __attribute__((ext_vector_type(4))) float f32x4;
typedef __attribute__((ext_vector_type(2))) float f32x2;
typedef __attribute__((ext_vector_type(16))) float f32x16;
typedef __attribute__((ext_vector_type(4))) unsigned u32x4;
typedef __attribute__((ext_vector_type(2))) unsigned u32x2;
typedef __attribute__((address_space(1))) unsigned gu32;
typedef __attribute__((address_space(1))) unsigned long long gu64;
#define LAS __attribute__((address_space(3)))
#define RLX_AGENT __ATOMIC_RELAXED, __HIP_MEMORY_SCOPE_AGENT
#define DEV __device__ __forceinline__

constexpr unsigned WS_FLAG = 4096;
constexpr unsigned WS_MOD = WS_FLAG + 4 * 256 * 128;
constexpr unsigned WS_KV = WS_MOD + 16384;
constexpr unsigned WS_ST = WS_KV + 256 * 4352;
constexpr unsigned WS_O = WS_ST + 2048 * 128;
constexpr unsigned WS_X1 = WS_O + 2097152;
constexpr unsigned WS_H = WS_X1 + 2097152;
constexpr unsigned WS_END = WS_H + 128 * 43 * 1024;
constexpr unsigned L_MOD = 147456, L_SCR = 159744;
constexpr int D3 = 10, D4 = 12;
constexpr unsigned SPIN_CAP = 1u << 19;

DEV unsigned pk2(float a, float b) { f32x2 v = {a, b}; return __builtin_bit_cast(unsigned, __builtin_convertvector(v, bf16x2)); }
DEV float lo16(unsigned u) { return __uint_as_float(u << 16); }
DEV float hi16(unsigned u) { return __uint_as_float(u & 0xffff0000u); }
DEV float rbf(float a) { return lo16(pk2(a, a)); }
DEV f32x16 mfma(u32x4 a, u32x4 b, f32x16 c) {
  return __builtin_amdgcn_mfma_f32_32x32x16_bf16(__builtin_bit_cast(bf16x8, a), __builtin_bit_cast(bf16x8, b), c, 0, 0, 0); }
DEV u32x4 pk8(const float* v) { return u32x4{pk2(v[0], v[1]), pk2(v[2], v[3]), pk2(v[4], v[5]), pk2(v[6], v[7])}; }
DEV void swap32(unsigned& a, unsigned& b) { auto s = __builtin_amdgcn_permlane32_swap(a, b, false, false); a = s[0]; b = s[1]; }
DEV float hsum(float x) { unsigned a = __float_as_uint(x), b = a; swap32(a, b); return __uint_as_float(a) + __uint_as_float(b); }
DEV float hmax(float x) { unsigned a = __float_as_uint(x), b = a; swap32(a, b); return fmaxf(__uint_as_float(a), __uint_as_float(b)); }
#define XRED(name, OP) DEV float name(float x) { \
  auto s = __builtin_amdgcn_permlane16_swap(__float_as_uint(x), __float_as_uint(x), false, false); \
  x = OP(__uint_as_float(s[0]), __uint_as_float(s[1])); \
  auto t = __builtin_amdgcn_permlane32_swap(__float_as_uint(x), __float_as_uint(x), false, false); \
  return OP(__uint_as_float(t[0]), __uint_as_float(t[1])); }
DEV float addf(float a, float b) { return a + b; }
XRED(xsum, addf)
template <int N> DEV float ror(float x) { return __int_as_float(__builtin_amdgcn_update_dpp(0, __float_as_int(x), 0x120 + N, 0xf, 0xf, false)); }

DEV void glds(const void* g, unsigned lds) {
  unsigned keep;
  asm volatile("s_mov_b32 %0, m0\n\ts_mov_b32 m0, %2\n\ts_nop 0\n\tglobal_load_lds_dwordx4 %1, off\n\ts_mov_b32 m0, %0"
               : "=&s"(keep) : "v"(g), "s"(lds) : "memory");
}
DEV void touch(unsigned& d, const void* g) { asm volatile("global_load_dword %0, %1, off sc1" : "+v"(d) : "v"(g) : "memory"); }
DEV void keep(unsigned& d) { asm volatile("" : "+v"(d)); }
DEV void wait_vm0() { asm volatile("s_waitcnt vmcnt(0)" ::: "memory"); }
DEV void bar() { asm volatile("s_waitcnt lgkmcnt(0)\n\ts_barrier" ::: "memory"); }
typedef LAS char* lp;
DEV lp opq(unsigned a) { asm volatile("" : "+v"(a)); return (lp)a; }
DEV u32x4 fragp(lp a, int o, int d) {
  u32x2 x = __builtin_bit_cast(u32x2, __builtin_amdgcn_ds_read_tr16_b64_v4i16((LAS s16x4*)(a + o)));
  u32x2 y = __builtin_bit_cast(u32x2, __builtin_amdgcn_ds_read_tr16_b64_v4i16((LAS s16x4*)(a + o + d)));
  return u32x4{x[0], x[1], y[0], y[1]}; }
DEV f32x4 ldp4(lp a, int o) { return *(LAS f32x4*)(a + o); }
template <int I, int N, class F> DEV void sfor(F f) { if constexpr (I < N) { f(std::integral_constant<int, I>{}); sfor<I + 1, N>(f); } }
#define SB0() __builtin_amdgcn_sched_barrier(0)
template <int NM, int ND, int NV> DEV void ilv() {
#pragma unroll
  for (int i = 0; i < NM; ++i) { __builtin_amdgcn_sched_group_barrier(0x008, 1, 0); __builtin_amdgcn_sched_group_barrier(0x100, ND, 0);
    if (NV) __builtin_amdgcn_sched_group_barrier(0x002, NV, 0); }
}
DEV f32x4 ldsf4(unsigned a) { return *(LAS f32x4*)a; }
DEV void stsf4(unsigned a, f32x4 v) { *(LAS f32x4*)a = v; }

DEV void wait_flags(char* ws, unsigned first, unsigned stride, unsigned n, unsigned tag, int wave, int lane) {
  if (wave == 0) {
    gu32* p = (gu32*)(ws + WS_FLAG) + (first + (lane & (n - 1)) * stride) * 32;
#pragma unroll 1
    for (unsigned s = 0; s < SPIN_CAP; ++s) {
      if (__all(__hip_atomic_load(p, RLX_AGENT) == tag)) break;
      __builtin_amdgcn_s_sleep(1);
    }
  }
  bar();
}
DEV void publish(char* ws, unsigned idx, unsigned tag) {
  wait_vm0(); bar();
  if (threadIdx.x < 32) __hip_atomic_store((gu32*)(ws + WS_FLAG) + idx * 32 + threadIdx.x, tag, RLX_AGENT);
}
DEV u32x4 ldx(__amdgpu_buffer_rsrc_t rs, unsigned v, unsigned s) { return __builtin_amdgcn_raw_buffer_load_b128(rs, v, s, 16); }
#define ST(i)
#define ST2(i)

DEV void hedgehog(u32x4 wp, const float* v, u32x4& o0, u32x4& o1) {
  const float L = 1.4426950408889634f; f32x16 Z = {0}; f32x16 pp = mfma(wp, pk8(v), Z); float e[16];
#pragma unroll
  for (int s = 0; s < 2; ++s) {
    float m = fmaxf(fmaxf(fmaxf(pp[8 * s], pp[8 * s + 1]), fmaxf(pp[8 * s + 2], pp[8 * s + 3])), fmaxf(fmaxf(pp[8 * s + 4], pp[8 * s + 5]), fmaxf(pp[8 * s + 6], pp[8 * s + 7])));
    m = -hmax(m) * L; float t = 0;
#pragma unroll
    for (int i = 0; i < 8; ++i) { e[8 * s + i] = __builtin_amdgcn_exp2f(pp[8 * s + i] * L + m); t += e[8 * s + i]; }
    t = __builtin_amdgcn_rcpf(hsum(t));
#pragma unroll
    for (int i = 0; i < 8; ++i) e[8 * s + i] *= t;
  }
  o0 = pk8(e); o1 = pk8(e + 8);
}
DEV void reduce4(unsigned base, const f32x16 (&acc)[3], int wave, int lane, float* r) {
  const int wm = wave & 1, wq = wave >> 1;
#pragma unroll
  for (int d = 0; d < 4; ++d) {
    const f32x16 v = acc[d >> 1]; const int o = (d & 1) * 8;
    if (d != wq) { const unsigned a = base + ((d * 2 + wm) * 3 + ((wq - d - 1) & 3)) * 2048 + lane * 16;
      stsf4(a, f32x4{v[o], v[o + 1], v[o + 2], v[o + 3]}); stsf4(a + 1024, f32x4{v[o + 4], v[o + 5], v[o + 6], v[o + 7]}); }
    else {
#pragma unroll
      for (int i = 0; i < 8; ++i) r[i] = v[o + i]; }
  }
  bar();
#pragma unroll
  for (int s = 0; s < 6; ++s) { const f32x4 v = ldsf4(base + wave * 6144 + s * 1024 + lane * 16);
#pragma unroll
    for (int i = 0; i < 4; ++i) r[(s & 1) * 4 + i] += v[i]; }
}

__global__ void __launch_bounds__(512, 2)
adaln_block(const unsigned short* __restrict__ x, const unsigned short* __restrict__ wqkv, const unsigned short* __restrict__ wphi,
            const unsigned short* __restrict__ wo, const unsigned short* __restrict__ wg, const unsigned short* __restrict__ wu,
            const unsigned short* __restrict__ wd, const unsigned short* __restrict__ cin, const unsigned short* __restrict__ wmod,
            unsigned short* __restrict__ out, char* ws) {
  __shared__ __attribute__((aligned(16))) char smem[163840];
  const unsigned L0 = (unsigned)(unsigned long long)(LAS char*)smem;
  const int tid = threadIdx.x, lane = tid & 63, wave = __builtin_amdgcn_readfirstlane(tid >> 6);
  const int h = lane >> 5, r32 = lane & 31, cb = (lane >> 4) & 1, q = (lane & 15) >> 2, p = lane & 3;
  const int b = blockIdx.x, xc = b & 7, j = b >> 3;
  const int mt = (xc >> 1) * 4 + (j & 3), nt = (xc & 1) * 8 + (j >> 2);
  const int Mt = xc * 4 + (j & 3), Nt = j >> 2;
  const int m3 = (xc >> 1) * 2 + (j & 1), nf = (xc & 1) * 16 + (j >> 1);
  const int wm = wave & 3, wk = wave >> 2, wm2 = wave & 1, wq = wave >> 1;
  const __amdgpu_buffer_rsrc_t rs = __builtin_amdgcn_make_buffer_rsrc(ws, 0, WS_END, 0x00020000);
  const unsigned wbase = L0 + wave * 1024;
  const f32x16 Z16 = {0};
  ST(0);
  unsigned tagv, cv[4]; u32x4 wv[4]; const int gb = xc * 12 + j;
  asm volatile("global_load_dword %0, %1, off sc1" : "=v"(tagv) : "v"(ws) : "memory");
  if (j < 12) {
#pragma unroll
    for (int i = 0; i < 4; ++i) { const int row = (tid >> 2) + 128 * i;
      asm volatile("global_load_dwordx4 %0, %2, off\n\tglobal_load_ushort %1, %3, off" : "=&v"(wv[i]), "=&v"(cv[i]) : "v"(wmod + row * 3072 + gb * 32 + p * 8), "v"(cin + row) : "memory"); } }
  unsigned pf = 0;
  touch(pf, x + (xc >> 1) * 262144 + (j * 128 + wave * 16 + (lane & 15)) * 64);
#pragma unroll
  for (int n = 0; n < 16; ++n) glds(x + (mt * 128 + wm * 32 + 2 * n + h) * 512 + wk * 256 + ((r32 ^ ((2 * n + h) & 15)) * 8), L0 + wave * 16384 + n * 1024);
  { const int l = r32 < 24 ? r32 : 0, r = l / 12, c = l - r * 12; touch(pf, wqkv + (j * 16 + wave * 2 + r) * 1536 + (c >> 2) * 512 + ((xc & 1) * 4 + (c & 3)) * 64); }
  asm volatile("s_waitcnt vmcnt(17)" : "+v"(tagv), "+v"(wv[0]), "+v"(wv[1]), "+v"(wv[2]), "+v"(wv[3]), "+v"(cv[0]), "+v"(cv[1]), "+v"(cv[2]), "+v"(cv[3]) :: "memory");
  u32x4 xa[32];
  const int u0 = nf < 22 ? 3 * nf : 2 * nf + 22, nu = nf < 22 ? 3 : 2, ul = u0 > 83 ? 83 : u0;
  const unsigned short *gsrc[3], *qsrc[3];
#pragma unroll
  for (int i = 0; i < 3; ++i) {
    const int c0 = i * 512 + tid, row = c0 / 12, cc = c0 - row * 12;
    qsrc[i] = wqkv + row * 1536 + (cc >> 2) * 512 + nt * 32 + (cc & 3) * 8;
  }
  auto load_gu = [&](int n0, int n1) {
#pragma unroll
    for (int n = n0; n < n1; ++n) glds(gsrc[n % 3] + (n / 3) * 128 * 1376, wbase + 49152 + n * 8192); };
  const int off8 = (wm2 * 8 + (lane >> 3)) * 512 + Nt * 64 + ((((lane >> 2) ^ (lane >> 4)) & 1) * 32) + p * 8;
  const int ks4 = wq * 22 - (wq == 3);
  auto load_wd = [&](int i0, int i1) {
#pragma unroll
    for (int i = i0; i < i1; ++i) glds(wd + off8 + (i == 21 && wq == 3 ? 85 : ks4 + i) * 8192, wbase + (i < 18 ? i : i - 18) * 8192); };

  auto load_wo = [&](int i0, int i1) {
#pragma unroll
    for (int i = i0; i < i1; ++i) glds(wo + off8 + (wq * 8 + i) * 8192, wbase + (i < 6 ? 98304 : 0) + i * 8192); };
  const unsigned tag = __builtin_amdgcn_readfirstlane(tagv) + 1u;
  if (j < 12) {
    f32x2 a[4] = {{0, 0}, {0, 0}, {0, 0}, {0, 0}};
#pragma unroll
    for (int i = 0; i < 4; ++i) {
      float c = lo16(cv[i]); c = rbf(c * __builtin_amdgcn_rcpf(1.f + __expf(-c)));
#pragma unroll
      for (int e = 0; e < 4; ++e) a[e] += c * f32x2{lo16(wv[i][e]), hi16(wv[i][e])};
    }
#pragma unroll
    for (int e = 0; e < 8; ++e) { float v = a[e >> 1][e & 1]; v += ror<4>(v); v += ror<8>(v); a[e >> 1][e & 1] = v; }
    if (q == 0) { const unsigned d = L0 + L_MOD + (tid >> 4) * 128 + p * 32;
      stsf4(d, f32x4{a[0][0], a[0][1], a[1][0], a[1][1]}); stsf4(d + 16, f32x4{a[2][0], a[2][1], a[3][0], a[3][1]}); }
    bar();
    if (wave == 0) { f32x2 s = {0, 0};
#pragma unroll
      for (int w = 0; w < 8; ++w) s += *(LAS f32x2*)(L0 + L_MOD + w * 512 + lane * 8);
      s[0] = xsum(s[0]); s[1] = xsum(s[1]);
      if (lane < 16) __hip_atomic_store((gu64*)(ws + WS_MOD) + gb * 16 + lane, ((unsigned long long)tag << 32) | pk2(s[0], s[1]), RLX_AGENT); } }
  asm volatile("s_waitcnt vmcnt(1)" ::: "memory");
  { const unsigned xb = L0 + wave * 16384 + r32 * 512 + ((h ^ (r32 & 15)) * 16);
#pragma unroll
    for (int st = 0; st < 16; ++st) xa[st] = *(LAS u32x4*)(xb ^ (st * 32)); }
  bar();
  ST(15);
  f32x2 t[64]; u32x4 g[4];
  auto ldg = [&]() {
#pragma unroll
    for (int i = 0; i < 4; ++i) g[i] = ldx(rs, WS_MOD + (wk * 256 + (lane & 15) * 16 + h * 8) * 4 + (i >> 1) * 2048 + (i & 1) * 16, 0); };
  { f32x2 sv = {0, 0}, vv = {0, 0};
#pragma unroll
    for (int s = 0; s < 4; ++s) {
#pragma unroll
      for (int i = 0; i < 3; ++i) glds(qsrc[i] + s * 128 * 1536, wbase + (s * 3 + i) * 8192);
      SB0();
#pragma unroll
      for (int i = 0; i < 16; ++i) { t[16 * s + i] = f32x2{lo16(xa[4 * s + (i >> 2)][i & 3]), hi16(xa[4 * s + (i >> 2)][i & 3])}; sv += t[16 * s + i]; vv += t[16 * s + i] * t[16 * s + i]; }
      SB0(); }
    ST(16);
    const float sh = hsum(sv[0] + sv[1]), vh = hsum(vv[0] + vv[1]);
    if (h == 0) *(LAS f32x2*)(L0 + L_SCR + 1024 + wave * 256 + r32 * 8) = f32x2{sh, vh};
    bar();
    ldg(); SB0();
    const f32x2 o = *(LAS f32x2*)(L0 + L_SCR + 1024 + (wave ^ 4) * 256 + r32 * 8);
    const float mean = (sh + o[0]) * (1.f / 512.f), rstd = rsqrtf(fmaxf((vh + o[1]) * (1.f / 512.f) - mean * mean, 0.f) + 1e-5f), dl = -mean * rstd;
#pragma unroll
    for (int i = 0; i < 64; ++i) { t[i] = t[i] * rstd + dl; asm volatile("" : "+v"(t[i])); } }
  ST(1);
  u32x4 wp;
  { const unsigned short* pp = wphi + (nt * 2 + wk) * 256 + 4 * h * 16 + (lane & 15); const unsigned sg = cb ? 0x80008000u : 0u;
#pragma unroll
    for (int e = 0; e < 4; ++e) { const int d = (e >> 1) * 8 + (e & 1) * 2; wp[e] = ((unsigned)pp[d * 16] | ((unsigned)pp[d * 16 + 16] << 16)) ^ sg; } }
  float ms[8], mc[8];
  {
#pragma unroll 1
    for (unsigned s = 0; s < SPIN_CAP; ++s) { bool ok = true;
#pragma unroll
      for (int i = 0; i < 4; ++i) ok = ok && g[i][1] == tag && g[i][3] == tag;
      if (__all(ok)) break;
      asm volatile("s_sleep 1" ::: "memory");
      ldg();
    }
#pragma unroll
    for (int i = 0; i < 4; ++i) { const unsigned u = g[i >> 1][(i & 1) * 2], v = g[2 + (i >> 1)][(i & 1) * 2], c = pk2(1.f + lo16(v), 1.f + hi16(v));
      ms[2 * i] = lo16(u); ms[2 * i + 1] = hi16(u); mc[2 * i] = lo16(c); mc[2 * i + 1] = hi16(c); } }
  asm volatile("s_waitcnt vmcnt(0)" : "+v"(wp), "+v"(pf) :: "memory"); bar();
  ST(2);
  { const int l = (wave - 2) * 64 + lane, sg = l / 12, o = (j * 32 + sg) >> 1;
    const int e = o * 1376 + (xc & 1) * 768 + (l - sg * 12) * 64;
    touch(pf, wave < 2 ? wo + (j * 128 + wave * 64 + lane) * 64 : ((sg & 1) ? wu : wg) + (e < 704510 ? e : 704510)); }
  f32x16 acc[3] = {Z16, Z16, Z16};
  { const lp wb = opq(L0 + (wk * 256 + h * 8 + q) * 192 + cb * 32 + p * 8);
    u32x4 w[2][3], hh[2];
    auto ldw = [&](int st, int c) { w[st & 1][c] = fragp(wb, st * 3072 + c * 64, 768); };
    auto mkh = [&](auto S, int e) {
      constexpr int st = S; float v0 = t[st * 4 + e][0], v1 = t[st * 4 + e][1];
      asm("v_mul_f32_dpp %0, %2, %0 row_newbcast:%6 row_mask:0xf bank_mask:0xf\n\tv_mul_f32_dpp %1, %3, %1 row_newbcast:%6 row_mask:0xf bank_mask:0xf\n\t"
          "v_add_f32_dpp %0, %4, %0 row_newbcast:%6 row_mask:0xf bank_mask:0xf\n\tv_add_f32_dpp %1, %5, %1 row_newbcast:%6 row_mask:0xf bank_mask:0xf"
          : "+v"(v0), "+v"(v1) : "v"(mc[2 * e]), "v"(mc[2 * e + 1]), "v"(ms[2 * e]), "v"(ms[2 * e + 1]), "i"(st));
      hh[st & 1][e] = pk2(v0, v1); };
#pragma unroll
    for (int e = 0; e < 8; ++e) asm volatile("" : "+v"(ms[e]), "+v"(mc[e]));
    asm volatile("s_nop 1"); SB0();
    ldw(0, 0); ldw(0, 1); ldw(0, 2);
#pragma unroll
    for (int e = 0; e < 4; ++e) mkh(std::integral_constant<int, 0>{}, e);
    SB0();
    sfor<0, 16>([&](auto S) { constexpr int st = S; std::integral_constant<int, (st + 1) & 15> N;
      acc[0] = mfma(w[st & 1][0], hh[st & 1], acc[0]); SB0();
      if (st < 15) { ldw(st + 1, 0); mkh(N, 0); mkh(N, 1); }
      SB0(); acc[1] = mfma(w[st & 1][1], hh[st & 1], acc[1]); SB0();
      if (st < 15) { ldw(st + 1, 1); mkh(N, 2); }
      SB0(); acc[2] = mfma(w[st & 1][2], hh[st & 1], acc[2]); SB0();
      if (st < 15) { ldw(st + 1, 2); mkh(N, 3); }
      if (st >= 5 && st < 11) load_wo(st - 5, st - 4);
      SB0(); }); }
  ST(3);
  u32x4 phq0, phq1, gm;
  { float v[3][8];
    bar(); load_wo(6, 8);
    auto send = [&](int o) {
#pragma unroll
      for (int s = 0; s < 6; ++s) { const f32x16 a = acc[s >> 1]; const int i = o + (s & 1) * 4; stsf4(L0 + wave * 6144 + s * 1024 + lane * 16, f32x4{a[i], a[i + 1], a[i + 2], a[i + 3]}); } };
    auto recv = [&](int o) {
#pragma unroll
      for (int s = 0; s < 6; ++s) { const f32x4 r = ldsf4(L0 + (wave ^ 4) * 6144 + s * 1024 + lane * 16);
#pragma unroll
        for (int i = 0; i < 4; ++i) v[s >> 1][(s & 1) * 4 + i] = acc[s >> 1][o + (s & 1) * 4 + i] + r[i]; } };
    if (wk) { send(0); bar(); recv(8); } else { send(8); bar(); recv(0); }
    ST(17);
    u32x4 k0, k1; hedgehog(wp, v[1], k0, k1);
    const u32x4 vb = pk8(v[2]);
    const unsigned img = L0 + 65536 + wave * 4096, wr = img + r32 * 104 + h * 8;
    *(LAS u32x2*)(wr) = u32x2{k0[0], k0[1]}; *(LAS u32x2*)(wr + 16) = u32x2{k0[2], k0[3]}; *(LAS u32x2*)(wr + 32) = u32x2{k1[0], k1[1]}; *(LAS u32x2*)(wr + 48) = u32x2{k1[2], k1[3]};
    *(LAS u32x2*)(wr + 64) = u32x2{vb[0], vb[1]}; *(LAS u32x2*)(wr + 80) = u32x2{vb[2], vb[3]};
    asm volatile("" ::: "memory");
    f32x16 kv = Z16; const lp ra = opq(img + (8 * h + q) * 104 + cb * 32 + p * 8), rb = opq(img + (8 * h + q) * 104 + 64 + p * 8);
#pragma unroll
    for (int s = 0; s < 2; ++s) { u32x4 bb = fragp(rb, s * 1664, 416);
#pragma unroll
      for (int e = 0; e < 4; ++e) bb[e] = cb ? 0x3f803f80u : bb[e];
      kv = mfma(fragp(ra, s * 1664, 416), bb, kv); }
    asm volatile("" ::: "memory");
    ST(18);
    if (r32 <= 16) {
#pragma unroll
      for (int c = 0; c < 4; ++c) stsf4(img + (c * 34 + h * 17 + r32) * 16, f32x4{kv[4 * c], kv[4 * c + 1], kv[4 * c + 2], kv[4 * c + 3]}); }
    bar();
    if (tid < 136) { const int c = tid / 34; const unsigned a = L0 + 65536 + (c >> 1) * 16384 + ((c & 1) * 68 + tid - c * 34) * 16;
      const f32x4 s = (ldsf4(a) + ldsf4(a + 4096)) + (ldsf4(a + 8192) + ldsf4(a + 12288)), r = (ldsf4(a + 544) + ldsf4(a + 4640)) + (ldsf4(a + 8736) + ldsf4(a + 12832));
      __builtin_amdgcn_raw_buffer_store_b128(u32x4{pk2(s[0], s[1]), pk2(s[2], s[3]), pk2(r[0], r[1]), pk2(r[2], r[3])}, rs, WS_KV + (nt * 16 + mt) * 2176 + tid * 16, 0, 16); }
    ST(20);
    publish(ws, mt * 16 + nt, tag); keep(pf);
    ST(4);
    gm = ldx(rs, WS_MOD + 4096 + tid * 16, 0);
    int tid2 = tid; asm volatile("" : "+v"(tid2));
#pragma unroll
    for (int i = 0; i < 3; ++i) { const int c0 = i * 512 + tid2, row = c0 / 12, cc = c0 - row * 12;
      gsrc[i] = ((cc & 2) ? wu : wg) + row * 1376 + (ul + (cc >> 2)) * 16 + (cc & 1) * 8; }
    hedgehog(wp, v[0], phq0, phq1); }
  const int tok2 = Mt * 64 + wm2 * 32 + r32, col2 = Nt * 64 + wq * 16;
  const u32x4 xr = *(const u32x4*)(x + tok2 * 512 + col2 + 8 * h);
  const unsigned wb8 = L0 + wq * 2048 + (4 * h + q) * 128 + cb * 32 + p * 8 + (q >> 1) * 64;
  u32x4 wf[8][2];
  { const lp wa[4] = {opq(wb8 + 98304), opq((wb8 ^ 64) + 98304), opq(wb8), opq(wb8 ^ 64)};
#pragma unroll
    for (int i = 0; i < 16; ++i) wf[i >> 1][i & 1] = fragp(wa[(i & 1) + (i < 12 ? 0 : 2)], (i >> 1) * 8192, 1024); }
#pragma unroll 1
  for (unsigned s = 0; s < SPIN_CAP; ++s) {
    if (__all(gm[1] == tag && gm[3] == tag)) break;
    asm volatile("s_sleep 1" ::: "memory");
    gm = ldx(rs, WS_MOD + 4096 + tid * 16, 0);
  }
  if (wq == 2) { gm[0] = pk2(1.f + lo16(gm[0]), 1.f + hi16(gm[0])); gm[2] = pk2(1.f + lo16(gm[2]), 1.f + hi16(gm[2])); }
  stsf4(L0 + L_MOD + 4096 + tid * 16, f32x4{lo16(gm[0]), hi16(gm[0]), lo16(gm[2]), hi16(gm[2])});
  if (wq == 1 || wq == 2) *(LAS u32x2*)(L0 + L_MOD + (2 - wq) * 1024 + (tid & 124) * 8 + (tid & 1) * 16 + (tid & 2) * 4) = u32x2{gm[0], gm[2]};
  load_gu(2, 6);
  wait_flags(ws, nt, 16, 16, tag, wave, lane);
  ST(5);
  { if (tid < 272) { u32x4 v[8]; const int hf = tid >= 136;
#pragma unroll
      for (int i = 0; i < 8; ++i) v[i] = ldx(rs, WS_KV + (nt * 16 + hf * 7) * 2176 + tid * 16, i * 2176);
      f32x2 s[4] = {{0, 0}, {0, 0}, {0, 0}, {0, 0}};
#pragma unroll
      for (int i = 0; i < 32; ++i) s[i & 3] += f32x2{lo16(v[i >> 2][i & 3]), hi16(v[i >> 2][i & 3])};
      stsf4(L0 + tid * 16, f32x4{s[0][0], s[0][1], s[1][0], s[1][1]}); stsf4(L0 + 4352 + tid * 16, f32x4{s[2][0], s[2][1], s[3][0], s[3][1]}); }
    bar();
    ST(22);
    const unsigned a = L0 + (wk * 68 + h * 17 + (r32 < 16 ? r32 : 16)) * 16; u32x4 ka[2];
#pragma unroll
    for (int i = 0; i < 4; ++i) { const unsigned o = a + (i >> 1) * 544 + (i & 1) * 4352; const f32x4 k = ldsf4(o) + ldsf4(o + 2176);
      ka[i >> 1][(i & 1) * 2] = pk2(k[0], k[1]); ka[i >> 1][(i & 1) * 2 + 1] = pk2(k[2], k[3]); }
    const f32x16 d = mfma(ka[1], phq1, mfma(ka[0], phq0, Z16));
    const float rd = __builtin_amdgcn_rcpf(d[8] + 1e-6f); float o[8];
#pragma unroll
    for (int i = 0; i < 8; ++i) o[i] = d[i] * rd;
    __builtin_amdgcn_raw_buffer_store_b128(pk8(o), rs, WS_O + ((mt * 4 + wm) * 32 + nt * 2 + wk) * 1024 + lane * 16, 0, 16);
    ST(24);
    publish(ws, 256 + mt * 16 + nt, tag); }
  ST(6);
  load_gu(0, 2); load_gu(6, 9);
  if (wave >= 2 && tid < 472) touch(pf, wd + (j * 344 + tid - 128) * 64);
  wait_flags(ws, 256 + (Mt >> 1) * 16, 1, 16, tag, wave, lane);
  if (b == 0 && tid == 0) __hip_atomic_store((gu32*)ws, tag, RLX_AGENT);
  ST(7);
  if (wave == 0) touch(pf, ws + WS_O + Mt * 65536 + (Nt * 64 + lane) * 128);
  u32x4 x1p;
  {
#pragma unroll
    for (int i = 0; i < 8; ++i) { xa[i] = ldx(rs, lane * 16, WS_O + ((Mt * 2 + wm2) * 32 + wq * 8 + i) * 1024); SB0(); }
    acc[0] = Z16; acc[1] = Z16;
#pragma unroll
    for (int i = 0; i < 8; ++i) { acc[0] = mfma(wf[i][0], xa[i], acc[0]); acc[1] = mfma(wf[i][1], xa[i], acc[1]); SB0(); }
    load_gu(9, 12);
    ST(25);
    float r[8]; reduce4(L0, acc, wave, lane, r);
    unsigned xs[4] = {xr[0], xr[1], xr[2], xr[3]}; swap32(xs[0], xs[2]); swap32(xs[1], xs[3]);
    const unsigned ma = L0 + L_MOD + (col2 + 4 * h) * 4; float y[8], s = 0, v = 0;
#pragma unroll
    for (int e = 0; e < 2; ++e) { const f32x4 ga = ldsf4(ma + 4096 + e * 32), ca = ldsf4(ma + 8192 + e * 32);
      x1p[2 * e] = pk2(lo16(xs[2 * e]) + ga[0] * r[4 * e], hi16(xs[2 * e]) + ga[1] * r[4 * e + 1]);
      x1p[2 * e + 1] = pk2(lo16(xs[2 * e + 1]) + ga[2] * r[4 * e + 2], hi16(xs[2 * e + 1]) + ga[3] * r[4 * e + 3]);
#pragma unroll
      for (int i = 0; i < 4; ++i) { r[4 * e + i] = (i & 1) ? hi16(x1p[2 * e + (i >> 1)]) : lo16(x1p[2 * e + (i >> 1)]); y[4 * e + i] = r[4 * e + i] * ca[i]; s += r[4 * e + i]; } }
    __builtin_amdgcn_raw_buffer_store_b128(pk8(y), rs, WS_X1 + ((Mt * 2 + wm2) * 32 + Nt * 4 + wq) * 1024 + lane * 16, 0, 16);
    s = hsum(s) * (1.f / 16.f);
#pragma unroll
    for (int i = 0; i < 8; ++i) v += (r[i] - s) * (r[i] - s);
    v = hsum(v);
    if (h == 0) *(LAS f32x2*)(L0 + L_SCR + wave * 256 + r32 * 8) = f32x2{s, v};
    bar();
    if (tid < 64) { f32x2 a[4];
#pragma unroll
      for (int i = 0; i < 4; ++i) a[i] = *(LAS f32x2*)(L0 + L_SCR + i * 512 + tid * 8);
      const float m = ((a[0][0] + a[1][0]) + (a[2][0] + a[3][0])) * 0.25f; float M = 0;
#pragma unroll
      for (int i = 0; i < 4; ++i) M += a[i][1] + 16.f * (a[i][0] - m) * (a[i][0] - m);
      __builtin_amdgcn_raw_buffer_store_b64(u32x2{__float_as_uint(m), __float_as_uint(M)}, rs, WS_ST + (((Mt * 2 + h) * 8 + Nt) * 32 + r32) * 8, 0, 16); }
    ST(26);
    publish(ws, 512 + Mt * 8 + Nt, tag); keep(pf); }
  ST(8);
  load_wd(0, 6);
  const unsigned wb3 = L0 + 49152 + (4 * h + q) * 192 + cb * 32 + p * 8;
  if (wave >= 2) { f32x16 d = Z16; u32x4 fa[16], fb[16];
    const lp wpt = opq(wb3 + (wq - 1) * 64 + wm2 * 49152), mp = opq(L0 + L_MOD + (r32 == 1) * 1024 + wm2 * 512 + h * 16);
#pragma unroll
    for (int s = 0; s < 16; ++s) { fa[s] = *(LAS u32x4*)(mp + s * 32); fb[s] = fragp(wpt, s * 3072, 1536); }
    SB0();
#pragma unroll
    for (int s = 0; s < 16; ++s) d = mfma(fa[s], fb[s], d);
    if (h == 0) *(LAS f32x2*)(L0 + L_SCR + (wave - 2) * 256 + r32 * 8) = f32x2{d[0], d[1]}; }
  ST(28);
  wait_flags(ws, 512 + m3 * 32, 1, 32, tag, wave, lane);
  ST(9);
  if (wave < 2) touch(pf, ws + WS_X1 + m3 * 262144 + ((j >> 1) * 128 + tid) * 128);
  { const int tt = m3 * 8 + wave;
    auto lda = [&](int s) { xa[s] = ldx(rs, lane * 16, WS_X1 + (tt * 32 + s) * 1024); };
#pragma unroll
    for (int s = 0; s < D3; ++s) { lda(s); SB0(); }
    u32x2 su[4];
#pragma unroll
    for (int i = 0; i < 4; ++i) su[i] = __builtin_amdgcn_raw_buffer_load_b64(rs, ((4 * h + i) * 32 + r32) * 8, WS_ST + tt * 2048, 16);
    unsigned uv[3][3];
#pragma unroll
    for (int T = 0; T < 3; ++T) { const f32x2 a = *(LAS f32x2*)(L0 + L_SCR + T * 512 + r32 * 8) + *(LAS f32x2*)(L0 + L_SCR + T * 512 + 256 + r32 * 8);
      const float uh = rbf(a[0]), vh = rbf(a[1]); uv[T][0] = pk2(uh, uh); uv[T][1] = pk2(a[0] - uh, vh); uv[T][2] = pk2(vh, a[1] - vh); }
    acc[0] = Z16; acc[1] = Z16; acc[2] = Z16;
    const lp wpp[2] = {opq(wb3), opq(wb3 + 49152)}; u32x4 w[2][3];
    auto ldw = [&](int s) {
#pragma unroll
      for (int c = 0; c < 3; ++c) w[s & 1][c] = fragp(wpp[s >> 4], (s & 15) * 3072 + c * 64, 1536); };
    ST2(21);
    ldw(0); SB0();
#pragma unroll
    for (int s = 0; s < 32; ++s) {
      if (s == 15) bar();
      if (s + D3 < 32) lda(s + D3);
      if (s == 26) { asm volatile("" : "+v"(xa[26]), "+v"(xa[27]), "+v"(xa[28]), "+v"(xa[29]), "+v"(xa[30]), "+v"(xa[31]), "+v"(su[0]), "+v"(su[1]), "+v"(su[2]), "+v"(su[3])); load_wd(6, 12); }
      if (s < 31) ldw(s + 1);
#pragma unroll
      for (int c = 0; c < 3; ++c) acc[c] = mfma(w[s & 1][c], xa[s], acc[c]);
      ilv<3, 2, 0>(); SB0();
    }
    ST(10);
    const f32x4 s0 = __builtin_bit_cast(f32x4, u32x4{su[0][0], su[0][1], su[1][0], su[1][1]}), s1 = __builtin_bit_cast(f32x4, u32x4{su[2][0], su[2][1], su[3][0], su[3][1]});
    const float mean = hsum((s0[0] + s0[2]) + (s1[0] + s1[2])) * 0.125f;
    const float d0 = s0[0] - mean, d1 = s0[2] - mean, d2 = s1[0] - mean, d3 = s1[2] - mean;
    const float var = hsum((s0[1] + s0[3]) + (s1[1] + s1[3]) + 64.f * (d0 * d0 + d1 * d1 + d2 * d2 + d3 * d3)) * (1.f / 512.f);
    const float rstd = rsqrtf(var + 1e-5f), sd = (var + 1e-5f) * rstd, ah = rbf(-mean), dh = rbf(sd), cg = -1.4426950408889634f * rstd;
    const u32x4 bx = {h ? 0u : pk2(ah, -mean - ah), h ? 0u : pk2(ah, dh), h ? 0u : pk2(sd - dh, dh), 0u};
#pragma unroll
    for (int T = 0; T < 3; ++T) acc[T] = mfma(u32x4{uv[T][0], uv[T][1], uv[T][2], 0u}, bx, acc[T]);
#pragma unroll
    for (int T = 0; T < 3; ++T) { float r[8];
#pragma unroll
      for (int i = 0; i < 8; ++i) r[i] = (acc[T][8 + i] * rstd) * __builtin_amdgcn_rcpf(1.f + __builtin_amdgcn_exp2f(cg * acc[T][i]));
      if (ul + T >= u0 && ul + T < u0 + nu) __builtin_amdgcn_raw_buffer_store_b128(pk8(r), rs, WS_H + (tt * 86 + ul + T) * 1024 + lane * 16, 0, 16);
    }
    ST(29);
    publish(ws, 768 + m3 * 32 + nf, tag); keep(pf); }
  ST(11);
  load_wd(12, 18);
  wait_flags(ws, 768 + (Mt >> 2) * 32, 1, 32, tag, wave, lane);
  ST(12);
  if (tid < 172) touch(pf, ws + WS_H + Mt * 176128 + (Nt * 172 + tid) * 128);
  { const unsigned hb = WS_H + ((Mt * 2 + wm2) * 86 + ks4) * 1024;
    auto lda = [&](int i) { xa[i] = ldx(rs, lane * 16, hb + (i == 21 && wq >= 2 ? 20 : i) * 1024); };
#pragma unroll
    for (int i = 0; i < D4; ++i) { lda(i); SB0(); }
    acc[0] = Z16; acc[1] = Z16;
    lp wpp[3][2]; u32x4 w[2][2];
#pragma unroll
    for (int i = 0; i < 6; ++i) wpp[i >> 1][i & 1] = opq((wb8 ^ ((i & 1) * 64)) + (i >> 1) * 49152);
    auto ldw = [&](int i) { const int sl = i < 18 ? i : i - 18; w[i & 1][0] = fragp(wpp[sl / 6][0], (sl % 6) * 8192, 1024); w[i & 1][1] = fragp(wpp[sl / 6][1], (sl % 6) * 8192, 1024); };
    ST2(23);
    ldw(0); SB0();
#pragma unroll
    for (int i = 0; i < 22; ++i) {
      if (i == 3) { bar(); load_wd(18, 22); }
      if (i + D4 < 22) lda(i + D4);
      if (i == 17) { wait_vm0(); keep(pf); bar(); }
      if (i < 21) ldw(i + 1);
      if (i == 21 && wq >= 2) xa[21] = u32x4{0, 0, 0, 0};
      acc[0] = mfma(w[i & 1][0], xa[i], acc[0]); acc[1] = mfma(w[i & 1][1], xa[i], acc[1]);
      ilv<2, 2, 0>(); SB0();
    }
    ST(13);
    float r[8]; reduce4(L0 + 49152, acc, wave, lane, r);
    unsigned o[4];
#pragma unroll
    for (int e = 0; e < 2; ++e) { const f32x4 gv = ldsf4(L0 + L_MOD + (2560 + col2 + 4 * h) * 4 + e * 32);
      o[2 * e] = pk2(lo16(x1p[2 * e]) + gv[0] * r[4 * e], hi16(x1p[2 * e]) + gv[1] * r[4 * e + 1]);
      o[2 * e + 1] = pk2(lo16(x1p[2 * e + 1]) + gv[2] * r[4 * e + 2], hi16(x1p[2 * e + 1]) + gv[3] * r[4 * e + 3]); }
    swap32(o[0], o[2]); swap32(o[1], o[3]);
    *(LAS u32x4*)(L0 + 131072 + (wm2 * 32 + r32) * 144 + wq * 32 + h * 16) = u32x4{o[0], o[1], o[2], o[3]};
    bar();
    __builtin_amdgcn_raw_buffer_store_b128(*(LAS u32x4*)(L0 + 131072 + (tid >> 3) * 144 + (tid & 7) * 16), __builtin_amdgcn_make_buffer_rsrc(out, 0, 2097152, 0x00020000),
                                           (Mt * 64 + (tid >> 3)) * 1024 + Nt * 128 + (tid & 7) * 16, 0, 19);
  }
  ST(14);
}

extern "C" void kernel_launch(void* const* d_in, const int* in_sizes, int n_in, void* d_out, int out_size, void* d_ws, size_t ws_size,
                              hipStream_t stream) {
  typedef const unsigned short* P;
  adaln_block<<<256, 512, 0, stream>>>((P)d_in[0], (P)d_in[1], (P)d_in[2], (P)d_in[3], (P)d_in[5], (P)d_in[6], (P)d_in[7], (P)d_in[8], (P)d_in[9],
                                       (unsigned short*)d_out, (char*)d_ws);
}
```

```cpp
#include <hip/hip_runtime.h>
#include <hip/hip_bf16.h>
#include <type_traits>

typedef __attribute__((ext_vector_type(8))) __bf16 bf16x8;
typedef __attribute__((ext_vector_type(2))) __bf16 bf16x2;
typedef __attribute__((ext_vector_type(4))) short s16x4;
typedef __attribute__((ext_vector_type(4))) float f32x4;
typedef __attribute__((ext_vector_type(2))) float f32x2;
typedef __attribute__((ext_vector_type(16))) float f32x16;
typedef __attribute__((ext_vector_type(4))) unsigned u32x4;
typedef __attribute__((ext_vector_type(2))) unsigned u32x2;
typedef __attribute__((address_space(1))) unsigned gu32;
typedef __attribute__((address_space(1))) unsigned long long gu64;
#define LAS __attribute__((address_space(3)))
#define RLX_AGENT __ATOMIC_RELAXED, __HIP_MEMORY_SCOPE_AGENT
#define DEV __device__ __forceinline__

constexpr unsigned WS_FLAG = 4096;
constexpr unsigned WS_MOD = WS_FLAG + 4 * 256 * 128;
constexpr unsigned WS_KV = WS_MOD + 16384;
constexpr unsigned WS_ST = WS_KV + 256 * 4352;
constexpr unsigned WS_O = WS_ST + 2048 * 128;
constexpr unsigned WS_X1 = WS_O + 2097152;
constexpr unsigned WS_H = WS_X1 + 2097152;
constexpr unsigned WS_END = WS_H + 128 * 43 * 1024;
constexpr unsigned L_MOD = 147456, L_SCR = 159744;
constexpr int D3 = 10, D4 = 12;
constexpr unsigned SPIN_CAP = 1u << 19;

DEV unsigned pk2(float a, float b) { f32x2 v = {a, b}; return __builtin_bit_cast(unsigned, __builtin_convertvector(v, bf16x2)); }
DEV float lo16(unsigned u) { return __uint_as_float(u << 16); }
DEV float hi16(unsigned u) { return __uint_as_float(u & 0xffff0000u); }
DEV float rbf(float a) { return lo16(pk2(a, a)); }
DEV f32x16 mfma(u32x4 a, u32x4 b, f32x16 c) {
  return __builtin_amdgcn_mfma_f32_32x32x16_bf16(__builtin_bit_cast(bf16x8, a), __builtin_bit_cast(bf16x8, b), c, 0, 0, 0); }
DEV u32x4 pk8(const float* v) { return u32x4{pk2(v[0], v[1]), pk2(v[2], v[3]), pk2(v[4], v[5]), pk2(v[6], v[7])}; }
DEV void swap32(unsigned& a, unsigned& b) { auto s = __builtin_amdgcn_permlane32_swap(a, b, false, false); a = s[0]; b = s[1]; }
DEV float hsum(float x) { unsigned a = __float_as_uint(x), b = a; swap32(a, b); return __uint_as_float(a) + __uint_as_float(b); }
DEV float hmax(float x) { unsigned a = __float_as_uint(x), b = a; swap32(a, b); return fmaxf(__uint_as_float(a), __uint_as_float(b)); }
#define XRED(name, OP) DEV float name(float x) { \
  auto s = __builtin_amdgcn_permlane16_swap(__float_as_uint(x), __float_as_uint(x), false, false); \
  x = OP(__uint_as_float(s[0]), __uint_as_float(s[1])); \
  auto t = __builtin_amdgcn_permlane32_swap(__float_as_uint(x), __float_as_uint(x), false, false); \
  return OP(__uint_as_float(t[0]), __uint_as_float(t[1])); }
DEV float addf(float a, float b) { return a + b; }
XRED(xsum, addf)
template <int N> DEV float ror(float x) { return __int_as_float(__builtin_amdgcn_update_dpp(0, __float_as_int(x), 0x120 + N, 0xf, 0xf, false)); }

DEV void glds(const void* g, unsigned lds) {
  unsigned keep;
  asm volatile("s_mov_b32 %0, m0\n\ts_mov_b32 m0, %2\n\ts_nop 0\n\tglobal_load_lds_dwordx4 %1, off\n\ts_mov_b32 m0, %0"
               : "=&s"(keep) : "v"(g), "s"(lds) : "memory");
}
DEV void touch(unsigned& d, const void* g) { asm volatile("global_load_dword %0, %1, off sc1" : "+v"(d) : "v"(g) : "memory"); }
DEV void keep(unsigned& d) { asm volatile("" : "+v"(d)); }
DEV void wait_vm0() { asm volatile("s_waitcnt vmcnt(0)" ::: "memory"); }
DEV void bar() { asm volatile("s_waitcnt lgkmcnt(0)\n\ts_barrier" ::: "memory"); }
typedef LAS char* lp;
DEV lp opq(unsigned a) { asm volatile("" : "+v"(a)); return (lp)a; }
DEV u32x4 fragp(lp a, int o, int d) {
  u32x2 x = __builtin_bit_cast(u32x2, __builtin_amdgcn_ds_read_tr16_b64_v4i16((LAS s16x4*)(a + o)));
  u32x2 y = __builtin_bit_cast(u32x2, __builtin_amdgcn_ds_read_tr16_b64_v4i16((LAS s16x4*)(a + o + d)));
  return u32x4{x[0], x[1], y[0], y[1]}; }
DEV f32x4 ldp4(lp a, int o) { return *(LAS f32x4*)(a + o); }
template <int I, int N, class F> DEV void sfor(F f) { if constexpr (I < N) { f(std::integral_constant<int, I>{}); sfor<I + 1, N>(f); } }
#define SB0() __builtin_amdgcn_sched_barrier(0)
template <int NM, int ND, int NV> DEV void ilv() {
#pragma unroll
  for (int i = 0; i < NM; ++i) { __builtin_amdgcn_sched_group_barrier(0x008, 1, 0); __builtin_amdgcn_sched_group_barrier(0x100, ND, 0);
    if (NV) __builtin_amdgcn_sched_group_barrier(0x002, NV, 0); }
}
DEV f32x4 ldsf4(unsigned a) { return *(LAS f32x4*)a; }
DEV void stsf4(unsigned a, f32x4 v) { *(LAS f32x4*)a = v; }

DEV void wait_flags(char* ws, unsigned first, unsigned stride, unsigned n, unsigned tag, int wave, int lane) {
  if (wave == 0) {
    gu32* p = (gu32*)(ws + WS_FLAG) + (first + (lane & (n - 1)) * stride) * 32;
#pragma unroll 1
    for (unsigned s = 0; s < SPIN_CAP; ++s) {
      if (__all(__hip_atomic_load(p, RLX_AGENT) == tag)) break;
      __builtin_amdgcn_s_sleep(1);
    }
  }
  bar();
}
DEV void publish(char* ws, unsigned idx, unsigned tag) {
  wait_vm0(); bar();
  if (threadIdx.x < 32) __hip_atomic_store((gu32*)(ws + WS_FLAG) + idx * 32 + threadIdx.x, tag, RLX_AGENT);
}
DEV u32x4 ldx(__amdgpu_buffer_rsrc_t rs, unsigned v, unsigned s) { return __builtin_amdgcn_raw_buffer_load_b128(rs, v, s, 16); }
#define ST(i)
#define ST2(i)

DEV void hedgehog(u32x4 wp, const float* v, u32x4& o0, u32x4& o1) {
  const float L = 1.4426950408889634f; f32x16 Z = {0}; f32x16 pp = mfma(wp, pk8(v), Z); float e[16];
#pragma unroll
  for (int s = 0; s < 2; ++s) {
    float m = fmaxf(fmaxf(fmaxf(pp[8 * s], pp[8 * s + 1]), fmaxf(pp[8 * s + 2], pp[8 * s + 3])), fmaxf(fmaxf(pp[8 * s + 4], pp[8 * s + 5]), fmaxf(pp[8 * s + 6], pp[8 * s + 7])));
    m = -hmax(m) * L; float t = 0;
#pragma unroll
    for (int i = 0; i < 8; ++i) { e[8 * s + i] = __builtin_amdgcn_exp2f(pp[8 * s + i] * L + m); t += e[8 * s + i]; }
    t = __builtin_amdgcn_rcpf(hsum(t));
#pragma unroll
    for (int i = 0; i < 8; ++i) e[8 * s + i] *= t;
  }
  o0 = pk8(e); o1 = pk8(e + 8);
}
DEV void reduce4(unsigned base, const f32x16 (&acc)[3], int wave, int lane, float* r) {
  const int wm = wave & 1, wq = wave >> 1;
#pragma unroll
  for (int d = 0; d < 4; ++d) {
    const f32x16 v = acc[d >> 1]; const int o = (d & 1) * 8;
    if (d != wq) { const unsigned a = base + ((d * 2 + wm) * 3 + ((wq - d - 1) & 3)) * 2048 + lane * 16;
      stsf4(a, f32x4{v[o], v[o + 1], v[o + 2], v[o + 3]}); stsf4(a + 1024, f32x4{v[o + 4], v[o + 5], v[o + 6], v[o + 7]}); }
    else {
#pragma unroll
      for (int i = 0; i < 8; ++i) r[i] = v[o + i]; }
  }
  bar();
#pragma unroll
  for (int s = 0; s < 6; ++s) { const f32x4 v = ldsf4(base + wave * 6144 + s * 1024 + lane * 16);
#pragma unroll
    for (int i = 0; i < 4; ++i) r[(s & 1) * 4 + i] += v[i]; }
}

__global__ void __launch_bounds__(512, 2)
adaln_block(const unsigned short* __restrict__ x, const unsigned short* __restrict__ wqkv, const unsigned short* __restrict__ wphi,
            const unsigned short* __restrict__ wo, const unsigned short* __restrict__ wg, const unsigned short* __restrict__ wu,
            const unsigned short* __restrict__ wd, const unsigned short* __restrict__ cin, const unsigned short* __restrict__ wmod,
            unsigned short* __restrict__ out, char* ws) {
  __shared__ __attribute__((aligned(16))) char smem[163840];
  const unsigned L0 = (unsigned)(unsigned long long)(LAS char*)smem;
  const int tid = threadIdx.x, lane = tid & 63, wave = __builtin_amdgcn_readfirstlane(tid >> 6);
  const int h = lane >> 5, r32 = lane & 31, cb = (lane >> 4) & 1, q = (lane & 15) >> 2, p = lane & 3;
  const int b = blockIdx.x, xc = b & 7, j = b >> 3;
  const int mt = (xc >> 1) * 4 + (j & 3), nt = (xc & 1) * 8 + (j >> 2);
  const int Mt = xc * 4 + (j & 3), Nt = j >> 2;
  const int m3 = (xc >> 1) * 2 + (j & 1), nf = (xc & 1) * 16 + (j >> 1);
  const int wm = wave & 3, wk = wave >> 2, wm2 = wave & 1, wq = wave >> 1;
  const __amdgpu_buffer_rsrc_t rs = __builtin_amdgcn_make_buffer_rsrc(ws, 0, WS_END, 0x00020000);
  const unsigned wbase = L0 + wave * 1024;
  const f32x16 Z16 = {0};
  ST(0);
  asm volatile("" :: "s"(wmod), "s"(cin), "s"(x), "s"(ws));
  unsigned tagv, cv[4]; u32x4 wv[4]; const int gb = xc * 12 + j;
  asm volatile("global_load_dword %0, %1, off sc1" : "=v"(tagv) : "v"(ws) : "memory");
  if (j < 12) {
#pragma unroll
    for (int i = 0; i < 4; ++i) { const int row = (tid >> 2) + 128 * i;
      asm volatile("global_load_dwordx4 %0, %2, off\n\tglobal_load_ushort %1, %3, off" : "=&v"(wv[i]), "=&v"(cv[i]) : "v"(wmod + row * 3072 + gb * 32 + p * 8), "v"(cin + row) : "memory"); } }
  unsigned pf = 0;
  touch(pf, x + (xc >> 1) * 262144 + (j * 128 + wave * 16 + (lane & 15)) * 64);
#pragma unroll
  for (int n = 0; n < 16; ++n) glds(x + (mt * 128 + wm * 32 + 2 * n + h) * 512 + wk * 256 + ((r32 ^ ((2 * n + h) & 15)) * 8), L0 + wave * 16384 + n * 1024);
  { const int l = r32 < 24 ? r32 : 0, r = l / 12, c = l - r * 12; touch(pf, wqkv + (j * 16 + wave * 2 + r) * 1536 + (c >> 2) * 512 + ((xc & 1) * 4 + (c & 3)) * 64); }
  asm volatile("s_waitcnt vmcnt(17)" : "+v"(tagv), "+v"(wv[0]), "+v"(wv[1]), "+v"(wv[2]), "+v"(wv[3]), "+v"(cv[0]), "+v"(cv[1]), "+v"(cv[2]), "+v"(cv[3]) :: "memory");
  u32x4 xa[32];
  const int u0 = nf < 22 ? 3 * nf : 2 * nf + 22, nu = nf < 22 ? 3 : 2, ul = u0 > 83 ? 83 : u0;
  const unsigned short *gsrc[3], *qsrc[3];
#pragma unroll
  for (int i = 0; i < 3; ++i) {
    const int c0 = i * 512 + tid, row = c0 / 12, cc = c0 - row * 12;
    qsrc[i] = wqkv + row * 1536 + (cc >> 2) * 512 + nt * 32 + (cc & 3) * 8;
  }
  auto load_gu = [&](int n0, int n1) {
#pragma unroll
    for (int n = n0; n < n1; ++n) glds(gsrc[n % 3] + (n / 3) * 128 * 1376, wbase + 49152 + n * 8192); };
  const int off8 = (wm2 * 8 + (lane >> 3)) * 512 + Nt * 64 + ((((lane >> 2) ^ (lane >> 4)) & 1) * 32) + p * 8;
  const int ks4 = wq * 22 - (wq == 3);
  auto load_wd = [&](int i0, int i1) {
#pragma unroll
    for (int i = i0; i < i1; ++i) glds(wd + off8 + (i == 21 && wq == 3 ? 85 : ks4 + i) * 8192, wbase + (i < 18 ? i : i - 18) * 8192); };

  auto load_wo = [&](int i0, int i1) {
#pragma unroll
    for (int i = i0; i < i1; ++i) glds(wo + off8 + (wq * 8 + i) * 8192, wbase + (i < 6 ? 98304 : 0) + i * 8192); };
  const unsigned tag = __builtin_amdgcn_readfirstlane(tagv) + 1u;
  if (j < 12) {
    f32x2 a[4] = {{0, 0}, {0, 0}, {0, 0}, {0, 0}};
#pragma unroll
    for (int i = 0; i < 4; ++i) {
      float c = lo16(cv[i]); c = rbf(c * __builtin_amdgcn_rcpf(1.f + __expf(-c)));
#pragma unroll
      for (int e = 0; e < 4; ++e) a[e] += c * f32x2{lo16(wv[i][e]), hi16(wv[i][e])};
    }
#pragma unroll
    for (int e = 0; e < 8; ++e) { float v = a[e >> 1][e & 1]; v += ror<4>(v); v += ror<8>(v); a[e >> 1][e & 1] = v; }
    if (q == 0) { const unsigned d = L0 + L_MOD + (tid >> 4) * 128 + p * 32;
      stsf4(d, f32x4{a[0][0], a[0][1], a[1][0], a[1][1]}); stsf4(d + 16, f32x4{a[2][0], a[2][1], a[3][0], a[3][1]}); }
    bar();
    if (wave == 0) { f32x2 s = {0, 0};
#pragma unroll
      for (int w = 0; w < 8; ++w) s += *(LAS f32x2*)(L0 + L_MOD + w * 512 + lane * 8);
      s[0] = xsum(s[0]); s[1] = xsum(s[1]);
      if (lane < 16) __hip_atomic_store((gu64*)(ws + WS_MOD) + gb * 16 + lane, ((unsigned long long)tag << 32) | pk2(s[0], s[1]), RLX_AGENT); } }
  asm volatile("s_waitcnt vmcnt(1)" ::: "memory");
  { const unsigned xb = L0 + wave * 16384 + r32 * 512 + ((h ^ (r32 & 15)) * 16);
#pragma unroll
    for (int st = 0; st < 16; ++st) xa[st] = *(LAS u32x4*)(xb ^ (st * 32)); }
  bar();
  ST(15);
  f32x2 t[64]; u32x4 g[4];
  auto ldg = [&]() {
#pragma unroll
    for (int i = 0; i < 4; ++i) g[i] = ldx(rs, WS_MOD + (wk * 256 + (lane & 15) * 16 + h * 8) * 4 + (i >> 1) * 2048 + (i & 1) * 16, 0); };
  { f32x2 sv = {0, 0}, vv = {0, 0};
#pragma unroll
    for (int s = 0; s < 4; ++s) {
#pragma unroll
      for (int i = 0; i < 3; ++i) if (s < 2) glds(qsrc[i] + s * 256 * 1536, wbase + (s * 6 + i) * 8192);
      SB0();
#pragma unroll
      for (int i = 0; i < 16; ++i) { t[16 * s + i] = f32x2{lo16(xa[4 * s + (i >> 2)][i & 3]), hi16(xa[4 * s + (i >> 2)][i & 3])}; sv += t[16 * s + i]; vv += t[16 * s + i] * t[16 * s + i]; }
      SB0(); }
    ST(16);
    const float sh = hsum(sv[0] + sv[1]), vh = hsum(vv[0] + vv[1]);
    if (h == 0) *(LAS f32x2*)(L0 + L_SCR + 1024 + wave * 256 + r32 * 8) = f32x2{sh, vh};
    bar();
    ldg(); SB0();
    const f32x2 o = *(LAS f32x2*)(L0 + L_SCR + 1024 + (wave ^ 4) * 256 + r32 * 8);
    const float mean = (sh + o[0]) * (1.f / 512.f), rstd = rsqrtf(fmaxf((vh + o[1]) * (1.f / 512.f) - mean * mean, 0.f) + 1e-5f), dl = -mean * rstd;
#pragma unroll
    for (int i = 0; i < 64; ++i) { t[i] = t[i] * rstd + dl; asm volatile("" : "+v"(t[i])); } }
  ST(1);
  u32x4 wp;
  { const unsigned short* pp = wphi + (nt * 2 + wk) * 256 + 4 * h * 16 + (lane & 15); const unsigned sg = cb ? 0x80008000u : 0u;
#pragma unroll
    for (int e = 0; e < 4; ++e) { const int d = (e >> 1) * 8 + (e & 1) * 2; wp[e] = ((unsigned)pp[d * 16] | ((unsigned)pp[d * 16 + 16] << 16)) ^ sg; } }
  float ms[8], mc[8];
  {
#pragma unroll 1
    for (unsigned s = 0; s < SPIN_CAP; ++s) { bool ok = true;
#pragma unroll
      for (int i = 0; i < 4; ++i) ok = ok && g[i][1] == tag && g[i][3] == tag;
      if (__all(ok)) break;
      asm volatile("s_sleep 1" ::: "memory");
      ldg();
    }
#pragma unroll
    for (int i = 0; i < 4; ++i) { const unsigned u = g[i >> 1][(i & 1) * 2], v = g[2 + (i >> 1)][(i & 1) * 2], c = pk2(1.f + lo16(v), 1.f + hi16(v));
      ms[2 * i] = lo16(u); ms[2 * i + 1] = hi16(u); mc[2 * i] = lo16(c); mc[2 * i + 1] = hi16(c); } }
  asm volatile("s_waitcnt vmcnt(0)" : "+v"(wp), "+v"(pf) :: "memory"); bar();
  ST(2);
#pragma unroll
  for (int i = 0; i < 6; ++i) glds(qsrc[i % 3] + ((i / 3) * 256 + 128) * 1536, wbase + ((i / 3) * 6 + 3 + i % 3) * 8192);
  { const int l = (wave - 2) * 64 + lane, sg = l / 12, o = (j * 32 + sg) >> 1;
    const int e = o * 1376 + (xc & 1) * 768 + (l - sg * 12) * 64;
    touch(pf, wave < 2 ? wo + (j * 128 + wave * 64 + lane) * 64 : ((sg & 1) ? wu : wg) + (e < 704510 ? e : 704510)); }
  f32x16 acc[3] = {Z16, Z16, Z16};
  { const lp wb = opq(L0 + (wk * 256 + h * 8 + q) * 192 + cb * 32 + p * 8);
    u32x4 w[2][3], hh[2];
    auto ldw = [&](int st, int c) { w[st & 1][c] = fragp(wb, st * 3072 + c * 64, 768); };
    auto mkh = [&](auto S, int e) {
      constexpr int st = S; float v0 = t[st * 4 + e][0], v1 = t[st * 4 + e][1];
      asm("v_mul_f32_dpp %0, %2, %0 row_newbcast:%6 row_mask:0xf bank_mask:0xf\n\tv_mul_f32_dpp %1, %3, %1 row_newbcast:%6 row_mask:0xf bank_mask:0xf\n\t"
          "v_add_f32_dpp %0, %4, %0 row_newbcast:%6 row_mask:0xf bank_mask:0xf\n\tv_add_f32_dpp %1, %5, %1 row_newbcast:%6 row_mask:0xf bank_mask:0xf"
          : "+v"(v0), "+v"(v1) : "v"(mc[2 * e]), "v"(mc[2 * e + 1]), "v"(ms[2 * e]), "v"(ms[2 * e + 1]), "i"(st));
      hh[st & 1][e] = pk2(v0, v1); };
#pragma unroll
    for (int e = 0; e < 8; ++e) asm volatile("" : "+v"(ms[e]), "+v"(mc[e]));
    asm volatile("s_nop 1"); SB0();
    ldw(0, 0); ldw(0, 1); ldw(0, 2);
#pragma unroll
    for (int e = 0; e < 4; ++e) mkh(std::integral_constant<int, 0>{}, e);
    SB0();
    sfor<0, 16>([&](auto S) { constexpr int st = S; std::integral_constant<int, (st + 1) & 15> N;
      acc[0] = mfma(w[st & 1][0], hh[st & 1], acc[0]); SB0();
      if (st == 7) { asm volatile("s_waitcnt vmcnt(3)" ::: "memory"); bar(); SB0(); }
      if (st < 15) { ldw(st + 1, 0); mkh(N, 0); mkh(N, 1); }
      SB0(); acc[1] = mfma(w[st & 1][1], hh[st & 1], acc[1]); SB0();
      if (st < 15) { ldw(st + 1, 1); mkh(N, 2); }
      SB0(); acc[2] = mfma(w[st & 1][2], hh[st & 1], acc[2]); SB0();
      if (st < 15) { ldw(st + 1, 2); mkh(N, 3); }
      if (st >= 5 && st < 11) load_wo(st - 5, st - 4);
      SB0(); }); }
  ST(3);
  u32x4 phq0, phq1, gm;
  { float v[3][8];
    bar(); load_wo(6, 8);
    auto send = [&](int o) {
#pragma unroll
      for (int s = 0; s < 6; ++s) { const f32x16 a = acc[s >> 1]; const int i = o + (s & 1) * 4; stsf4(L0 + wave * 6144 + s * 1024 + lane * 16, f32x4{a[i], a[i + 1], a[i + 2], a[i + 3]}); } };
    auto recv = [&](int o) {
#pragma unroll
      for (int s = 0; s < 6; ++s) { const f32x4 r = ldsf4(L0 + (wave ^ 4) * 6144 + s * 1024 + lane * 16);
#pragma unroll
        for (int i = 0; i < 4; ++i) v[s >> 1][(s & 1) * 4 + i] = acc[s >> 1][o + (s & 1) * 4 + i] + r[i]; } };
    if (wk) { send(0); bar(); recv(8); } else { send(8); bar(); recv(0); }
    ST(17);
    u32x4 k0, k1; hedgehog(wp, v[1], k0, k1);
    const u32x4 vb = pk8(v[2]);
    const unsigned img = L0 + 65536 + wave * 4096, wr = img + r32 * 104 + h * 8;
    *(LAS u32x2*)(wr) = u32x2{k0[0], k0[1]}; *(LAS u32x2*)(wr + 16) = u32x2{k0[2], k0[3]}; *(LAS u32x2*)(wr + 32) = u32x2{k1[0], k1[1]}; *(LAS u32x2*)(wr + 48) = u32x2{k1[2], k1[3]};
    *(LAS u32x2*)(wr + 64) = u32x2{vb[0], vb[1]}; *(LAS u32x2*)(wr + 80) = u32x2{vb[2], vb[3]};
    asm volatile("" ::: "memory");
    f32x16 kv = Z16; const lp ra = opq(img + (8 * h + q) * 104 + cb * 32 + p * 8), rb = opq(img + (8 * h + q) * 104 + 64 + p * 8);
#pragma unroll
    for (int s = 0; s < 2; ++s) { u32x4 bb = fragp(rb, s * 1664, 416);
#pragma unroll
      for (int e = 0; e < 4; ++e) bb[e] = cb ? 0x3f803f80u : bb[e];
      kv = mfma(fragp(ra, s * 1664, 416), bb, kv); }
    asm volatile("" ::: "memory");
    ST(18);
    if (r32 <= 16) {
#pragma unroll
      for (int c = 0; c < 4; ++c) stsf4(img + (c * 34 + h * 17 + r32) * 16, f32x4{kv[4 * c], kv[4 * c + 1], kv[4 * c + 2], kv[4 * c + 3]}); }
    bar();
    if (tid < 136) { const int c = tid / 34; const unsigned a = L0 + 65536 + (c >> 1) * 16384 + ((c & 1) * 68 + tid - c * 34) * 16;
      const f32x4 s = (ldsf4(a) + ldsf4(a + 4096)) + (ldsf4(a + 8192) + ldsf4(a + 12288)), r = (ldsf4(a + 544) + ldsf4(a + 4640)) + (ldsf4(a + 8736) + ldsf4(a + 12832));
      __builtin_amdgcn_raw_buffer_store_b128(u32x4{pk2(s[0], s[1]), pk2(s[2], s[3]), pk2(r[0], r[1]), pk2(r[2], r[3])}, rs, WS_KV + (nt * 16 + mt) * 2176 + tid * 16, 0, 16); }
    ST(20);
    publish(ws, mt * 16 + nt, tag); keep(pf);
    ST(4);
    gm = ldx(rs, WS_MOD + 4096 + tid * 16, 0);
    int tid2 = tid; asm volatile("" : "+v"(tid2));
#pragma unroll
    for (int i = 0; i < 3; ++i) { const int c0 = i * 512 + tid2, row = c0 / 12, cc = c0 - row * 12;
      gsrc[i] = ((cc & 2) ? wu : wg) + row * 1376 + (ul + (cc >> 2)) * 16 + (cc & 1) * 8; }
    hedgehog(wp, v[0], phq0, phq1); }
  const int tok2 = Mt * 64 + wm2 * 32 + r32, col2 = Nt * 64 + wq * 16;
  const u32x4 xr = *(const u32x4*)(x + tok2 * 512 + col2 + 8 * h);
  const unsigned wb8 = L0 + wq * 2048 + (4 * h + q) * 128 + cb * 32 + p * 8 + (q >> 1) * 64;
  u32x4 wf[8][2];
  { const lp wa[4] = {opq(wb8 + 98304), opq((wb8 ^ 64) + 98304), opq(wb8), opq(wb8 ^ 64)};
#pragma unroll
    for (int i = 0; i < 16; ++i) wf[i >> 1][i & 1] = fragp(wa[(i & 1) + (i < 12 ? 0 : 2)], (i >> 1) * 8192, 1024); }
#pragma unroll 1
  for (unsigned s = 0; s < SPIN_CAP; ++s) {
    if (__all(gm[1] == tag && gm[3] == tag)) break;
    asm volatile("s_sleep 1" ::: "memory");
    gm = ldx(rs, WS_MOD + 4096 + tid * 16, 0);
  }
  if (wq == 2) { gm[0] = pk2(1.f + lo16(gm[0]), 1.f + hi16(gm[0])); gm[2] = pk2(1.f + lo16(gm[2]), 1.f + hi16(gm[2])); }
  stsf4(L0 + L_MOD + 4096 + tid * 16, f32x4{lo16(gm[0]), hi16(gm[0]), lo16(gm[2]), hi16(gm[2])});
  if (wq == 1 || wq == 2) *(LAS u32x2*)(L0 + L_MOD + (2 - wq) * 1024 + (tid & 124) * 8 + (tid & 1) * 16 + (tid & 2) * 4) = u32x2{gm[0], gm[2]};
  load_gu(2, 6);
  wait_flags(ws, nt, 16, 16, tag, wave, lane);
  ST(5);
  { if (tid < 272) { u32x4 v[8]; const int hf = tid >= 136;
#pragma unroll
      for (int i = 0; i < 8; ++i) v[i] = ldx(rs, WS_KV + (nt * 16 + hf * 7) * 2176 + tid * 16, i * 2176);
      f32x2 s[4] = {{0, 0}, {0, 0}, {0, 0}, {0, 0}};
#pragma unroll
      for (int i = 0; i < 32; ++i) s[i & 3] += f32x2{lo16(v[i >> 2][i & 3]), hi16(v[i >> 2][i & 3])};
      stsf4(L0 + tid * 16, f32x4{s[0][0], s[0][1], s[1][0], s[1][1]}); stsf4(L0 + 4352 + tid * 16, f32x4{s[2][0], s[2][1], s[3][0], s[3][1]}); }
    bar();
    ST(22);
    const unsigned a = L0 + (wk * 68 + h * 17 + (r32 < 16 ? r32 : 16)) * 16; u32x4 ka[2];
#pragma unroll
    for (int i = 0; i < 4; ++i) { const unsigned o = a + (i >> 1) * 544 + (i & 1) * 4352; const f32x4 k = ldsf4(o) + ldsf4(o + 2176);
      ka[i >> 1][(i & 1) * 2] = pk2(k[0], k[1]); ka[i >> 1][(i & 1) * 2 + 1] = pk2(k[2], k[3]); }
    const f32x16 d = mfma(ka[1], phq1, mfma(ka[0], phq0, Z16));
    const float rd = __builtin_amdgcn_rcpf(d[8] + 1e-6f); float o[8];
#pragma unroll
    for (int i = 0; i < 8; ++i) o[i] = d[i] * rd;
    __builtin_amdgcn_raw_buffer_store_b128(pk8(o), rs, WS_O + ((mt * 4 + wm) * 32 + nt * 2 + wk) * 1024 + lane * 16, 0, 16);
    ST(24);
    publish(ws, 256 + mt * 16 + nt, tag); }
  ST(6);
  load_gu(0, 2); load_gu(6, 9);
  if (wave >= 2 && tid < 472) touch(pf, wd + (j * 344 + tid - 128) * 64);
  wait_flags(ws, 256 + (Mt >> 1) * 16, 1, 16, tag, wave, lane);
  if (b == 0 && tid == 0) __hip_atomic_store((gu32*)ws, tag, RLX_AGENT);
  ST(7);
  if (wave == 0) touch(pf, ws + WS_O + Mt * 65536 + (Nt * 64 + lane) * 128);
  u32x4 x1p;
  {
#pragma unroll
    for (int i = 0; i < 8; ++i) { xa[i] = ldx(rs, lane * 16, WS_O + ((Mt * 2 + wm2) * 32 + wq * 8 + i) * 1024); SB0(); }
    acc[0] = Z16; acc[1] = Z16;
#pragma unroll
    for (int i = 0; i < 8; ++i) { acc[0] = mfma(wf[i][0], xa[i], acc[0]); acc[1] = mfma(wf[i][1], xa[i], acc[1]); SB0(); }
    load_gu(9, 12);
    ST(25);
    float r[8]; reduce4(L0, acc, wave, lane, r);
    unsigned xs[4] = {xr[0], xr[1], xr[2], xr[3]}; swap32(xs[0], xs[2]); swap32(xs[1], xs[3]);
    const unsigned ma = L0 + L_MOD + (col2 + 4 * h) * 4; float y[8], s = 0, v = 0;
#pragma unroll
    for (int e = 0; e < 2; ++e) { const f32x4 ga = ldsf4(ma + 4096 + e * 32), ca = ldsf4(ma + 8192 + e * 32);
      x1p[2 * e] = pk2(lo16(xs[2 * e]) + ga[0] * r[4 * e], hi16(xs[2 * e]) + ga[1] * r[4 * e + 1]);
      x1p[2 * e + 1] = pk2(lo16(xs[2 * e + 1]) + ga[2] * r[4 * e + 2], hi16(xs[2 * e + 1]) + ga[3] * r[4 * e + 3]);
#pragma unroll
      for (int i = 0; i < 4; ++i) { r[4 * e + i] = (i & 1) ? hi16(x1p[2 * e + (i >> 1)]) : lo16(x1p[2 * e + (i >> 1)]); y[4 * e + i] = r[4 * e + i] * ca[i]; s += r[4 * e + i]; } }
    __builtin_amdgcn_raw_buffer_store_b128(pk8(y), rs, WS_X1 + ((Mt * 2 + wm2) * 32 + Nt * 4 + wq) * 1024 + lane * 16, 0, 16);
    s = hsum(s) * (1.f / 16.f);
#pragma unroll
    for (int i = 0; i < 8; ++i) v += (r[i] - s) * (r[i] - s);
    v = hsum(v);
    if (h == 0) *(LAS f32x2*)(L0 + L_SCR + wave * 256 + r32 * 8) = f32x2{s, v};
    bar();
    if (tid < 64) { f32x2 a[4];
#pragma unroll
      for (int i = 0; i < 4; ++i) a[i] = *(LAS f32x2*)(L0 + L_SCR + i * 512 + tid * 8);
      const float m = ((a[0][0] + a[1][0]) + (a[2][0] + a[3][0])) * 0.25f; float M = 0;
#pragma unroll
      for (int i = 0; i < 4; ++i) M += a[i][1] + 16.f * (a[i][0] - m) * (a[i][0] - m);
      __builtin_amdgcn_raw_buffer_store_b64(u32x2{__float_as_uint(m), __float_as_uint(M)}, rs, WS_ST + (((Mt * 2 + h) * 8 + Nt) * 32 + r32) * 8, 0, 16); }
    ST(26);
    publish(ws, 512 + Mt * 8 + Nt, tag); keep(pf); }
  ST(8);
  load_wd(0, 6);
  const unsigned wb3 = L0 + 49152 + (4 * h + q) * 192 + cb * 32 + p * 8;
  if (wave >= 2) { f32x16 d = Z16; u32x4 fa[16], fb[16];
    const lp wpt = opq(wb3 + (wq - 1) * 64 + wm2 * 49152), mp = opq(L0 + L_MOD + (r32 == 1) * 1024 + wm2 * 512 + h * 16);
#pragma unroll
    for (int s = 0; s < 16; ++s) { fa[s] = *(LAS u32x4*)(mp + s * 32); fb[s] = fragp(wpt, s * 3072, 1536); }
    SB0();
#pragma unroll
    for (int s = 0; s < 16; ++s) d = mfma(fa[s], fb[s], d);
    if (h == 0) *(LAS f32x2*)(L0 + L_SCR + (wave - 2) * 256 + r32 * 8) = f32x2{d[0], d[1]}; }
  ST(28);
  wait_flags(ws, 512 + m3 * 32, 1, 32, tag, wave, lane);
  ST(9);
  if (wave < 2) touch(pf, ws + WS_X1 + m3 * 262144 + ((j >> 1) * 128 + tid) * 128);
  { const int tt = m3 * 8 + wave;
    auto lda = [&](int s) { xa[s] = ldx(rs, lane * 16, WS_X1 + (tt * 32 + s) * 1024); };
#pragma unroll
    for (int s = 0; s < D3; ++s) { lda(s); SB0(); }
    u32x2 su[4];
#pragma unroll
    for (int i = 0; i < 4; ++i) su[i] = __builtin_amdgcn_raw_buffer_load_b64(rs, ((4 * h + i) * 32 + r32) * 8, WS_ST + tt * 2048, 16);
    unsigned uv[3][3];
#pragma unroll
    for (int T = 0; T < 3; ++T) { const f32x2 a = *(LAS f32x2*)(L0 + L_SCR + T * 512 + r32 * 8) + *(LAS f32x2*)(L0 + L_SCR + T * 512 + 256 + r32 * 8);
      const float uh = rbf(a[0]), vh = rbf(a[1]); uv[T][0] = pk2(uh, uh); uv[T][1] = pk2(a[0] - uh, vh); uv[T][2] = pk2(vh, a[1] - vh); }
    acc[0] = Z16; acc[1] = Z16; acc[2] = Z16;
    const lp wpp[2] = {opq(wb3), opq(wb3 + 49152)}; u32x4 w[2][3];
    auto ldw = [&](int s) {
#pragma unroll
      for (int c = 0; c < 3; ++c) w[s & 1][c] = fragp(wpp[s >> 4], (s & 15) * 3072 + c * 64, 1536); };
    ST2(21);
    ldw(0); SB0();
#pragma unroll
    for (int s = 0; s < 32; ++s) {
      if (s == 15) bar();
      if (s + D3 < 32) lda(s + D3);
      if (s == 26) { asm volatile("" : "+v"(xa[26]), "+v"(xa[27]), "+v"(xa[28]), "+v"(xa[29]), "+v"(xa[30]), "+v"(xa[31]), "+v"(su[0]), "+v"(su[1]), "+v"(su[2]), "+v"(su[3])); load_wd(6, 12); }
      if (s < 31) ldw(s + 1);
#pragma unroll
      for (int c = 0; c < 3; ++c) acc[c] = mfma(w[s & 1][c], xa[s], acc[c]);
      ilv<3, 2, 0>(); SB0();
    }
    ST(10);
    const f32x4 s0 = __builtin_bit_cast(f32x4, u32x4{su[0][0], su[0][1], su[1][0], su[1][1]}), s1 = __builtin_bit_cast(f32x4, u32x4{su[2][0], su[2][1], su[3][0], su[3][1]});
    const float mean = hsum((s0[0] + s0[2]) + (s1[0] + s1[2])) * 0.125f;
    const float d0 = s0[0] - mean, d1 = s0[2] - mean, d2 = s1[0] - mean, d3 = s1[2] - mean;
    const float var = hsum((s0[1] + s0[3]) + (s1[1] + s1[3]) + 64.f * (d0 * d0 + d1 * d1 + d2 * d2 + d3 * d3)) * (1.f / 512.f);
    const float rstd = rsqrtf(var + 1e-5f), sd = (var + 1e-5f) * rstd, ah = rbf(-mean), dh = rbf(sd), cg = -1.4426950408889634f * rstd;
    const u32x4 bx = {h ? 0u : pk2(ah, -mean - ah), h ? 0u : pk2(ah, dh), h ? 0u : pk2(sd - dh, dh), 0u};
#pragma unroll
    for (int T = 0; T < 3; ++T) acc[T] = mfma(u32x4{uv[T][0], uv[T][1], uv[T][2], 0u}, bx, acc[T]);
#pragma unroll
    for (int T = 0; T < 3; ++T) { float r[8];
#pragma unroll
      for (int i = 0; i < 8; ++i) r[i] = (acc[T][8 + i] * rstd) * __builtin_amdgcn_rcpf(1.f + __builtin_amdgcn_exp2f(cg * acc[T][i]));
      if (ul + T >= u0 && ul + T < u0 + nu) __builtin_amdgcn_raw_buffer_store_b128(pk8(r), rs, WS_H + (tt * 86 + ul + T) * 1024 + lane * 16, 0, 16);
    }
    ST(29);
    publish(ws, 768 + m3 * 32 + nf, tag); keep(pf); }
  ST(11);
  load_wd(12, 18);
  wait_flags(ws, 768 + (Mt >> 2) * 32, 1, 32, tag, wave, lane);
  ST(12);
  if (tid < 172) touch(pf, ws + WS_H + Mt * 176128 + (Nt * 172 + tid) * 128);
  { const unsigned hb = WS_H + ((Mt * 2 + wm2) * 86 + ks4) * 1024;
    auto lda = [&](int i) { xa[i] = ldx(rs, lane * 16, hb + (i == 21 && wq >= 2 ? 20 : i) * 1024); };
#pragma unroll
    for (int i = 0; i < D4; ++i) { lda(i); SB0(); }
    acc[0] = Z16; acc[1] = Z16;
    lp wpp[3][2]; u32x4 w[2][2];
#pragma unroll
    for (int i = 0; i < 6; ++i) wpp[i >> 1][i & 1] = opq((wb8 ^ ((i & 1) * 64)) + (i >> 1) * 49152);
    auto ldw = [&](int i) { const int sl = i < 18 ? i : i - 18; w[i & 1][0] = fragp(wpp[sl / 6][0], (sl % 6) * 8192, 1024); w[i & 1][1] = fragp(wpp[sl / 6][1], (sl % 6) * 8192, 1024); };
    ST2(23);
    ldw(0); SB0();
#pragma unroll
    for (int i = 0; i < 22; ++i) {
      if (i == 3) { bar(); load_wd(18, 22); }
      if (i + D4 < 22) lda(i + D4);
      if (i == 17) { wait_vm0(); keep(pf); bar(); }
      if (i < 21) ldw(i + 1);
      if (i == 21 && wq >= 2) xa[21] = u32x4{0, 0, 0, 0};
      acc[0] = mfma(w[i & 1][0], xa[i], acc[0]); acc[1] = mfma(w[i & 1][1], xa[i], acc[1]);
      ilv<2, 2, 0>(); SB0();
    }
    ST(13);
    float r[8]; reduce4(L0 + 49152, acc, wave, lane, r);
    unsigned o[4];
#pragma unroll
    for (int e = 0; e < 2; ++e) { const f32x4 gv = ldsf4(L0 + L_MOD + (2560 + col2 + 4 * h) * 4 + e * 32);
      o[2 * e] = pk2(lo16(x1p[2 * e]) + gv[0] * r[4 * e], hi16(x1p[2 * e]) + gv[1] * r[4 * e + 1]);
      o[2 * e + 1] = pk2(lo16(x1p[2 * e + 1]) + gv[2] * r[4 * e + 2], hi16(x1p[2 * e + 1]) + gv[3] * r[4 * e + 3]); }
    swap32(o[0], o[2]); swap32(o[1], o[3]);
    *(LAS u32x4*)(L0 + 131072 + (wm2 * 32 + r32) * 144 + wq * 32 + h * 16) = u32x4{o[0], o[1], o[2], o[3]};
    bar();
    __builtin_amdgcn_raw_buffer_store_b128(*(LAS u32x4*)(L0 + 131072 + (tid >> 3) * 144 + (tid & 7) * 16), __builtin_amdgcn_make_buffer_rsrc(out, 0, 2097152, 0x00020000),
                                           (Mt * 64 + (tid >> 3)) * 1024 + Nt * 128 + (tid & 7) * 16, 0, 19);
  }
  ST(14);
}

extern "C" void kernel_launch(void* const* d_in, const int* in_sizes, int n_in, void* d_out, int out_size, void* d_ws, size_t ws_size,
                              hipStream_t stream) {
  typedef const unsigned short* P;
  adaln_block<<<256, 512, 0, stream>>>((P)d_in[0], (P)d_in[1], (P)d_in[2], (P)d_in[3], (P)d_in[5], (P)d_in[6], (P)d_in[7], (P)d_in[8], (P)d_in[9],
                                       (unsigned short*)d_out, (char*)d_ws);
}
```

```cpp
#include <hip/hip_runtime.h>
#include <hip/hip_bf16.h>
#include <type_traits>

typedef __attribute__((ext_vector_type(8))) __bf16 bf16x8;
typedef __attribute__((ext_vector_type(2))) __bf16 bf16x2;
typedef __attribute__((ext_vector_type(4))) short s16x4;
typedef __attribute__((ext_vector_type(4))) float f32x4;
typedef __attribute__((ext_vector_type(2))) float f32x2;
typedef __attribute__((ext_vector_type(16))) float f32x16;
typedef __attribute__((ext_vector_type(4))) unsigned u32x4;
typedef __attribute__((ext_vector_type(2))) unsigned u32x2;
typedef __attribute__((address_space(1))) unsigned gu32;
typedef __attribute__((address_space(1))) unsigned long long gu64;
#define LAS __attribute__((address_space(3)))
#define RLX_AGENT __ATOMIC_RELAXED, __HIP_MEMORY_SCOPE_AGENT
#define DEV __device__ __forceinline__

constexpr unsigned WS_FLAG = 4096;
constexpr unsigned WS_MOD = WS_FLAG + 4 * 256 * 128;
constexpr unsigned WS_KV = WS_MOD + 16384;
constexpr unsigned WS_ST = WS_KV + 256 * 4352;
constexpr unsigned WS_O = WS_ST + 2048 * 128;
constexpr unsigned WS_X1 = WS_O + 2097152;
constexpr unsigned WS_H = WS_X1 + 2097152;
constexpr unsigned WS_END = WS_H + 128 * 43 * 1024;
constexpr unsigned L_MOD = 147456, L_SCR = 159744;
constexpr int D3 = 7, D4 = 7;
constexpr unsigned SPIN_CAP = 1u << 19;

DEV unsigned pk2(float a, float b) { f32x2 v = {a, b}; return __builtin_bit_cast(unsigned, __builtin_convertvector(v, bf16x2)); }
DEV float lo16(unsigned u) { return __uint_as_float(u << 16); }
DEV float hi16(unsigned u) { return __uint_as_float(u & 0xffff0000u); }
DEV float rbf(float a) { return lo16(pk2(a, a)); }
DEV f32x16 mfma(u32x4 a, u32x4 b, f32x16 c) {
  return __builtin_amdgcn_mfma_f32_32x32x16_bf16(__builtin_bit_cast(bf16x8, a), __builtin_bit_cast(bf16x8, b), c, 0, 0, 0); }
DEV u32x4 pk8(const float* v) { return u32x4{pk2(v[0], v[1]), pk2(v[2], v[3]), pk2(v[4], v[5]), pk2(v[6], v[7])}; }
DEV void swap32(unsigned& a, unsigned& b) { auto s = __builtin_amdgcn_permlane32_swap(a, b, false, false); a = s[0]; b = s[1]; }
DEV float hsum(float x) { unsigned a = __float_as_uint(x), b = a; swap32(a, b); return __uint_as_float(a) + __uint_as_float(b); }
DEV float hmax(float x) { unsigned a = __float_as_uint(x), b = a; swap32(a, b); return fmaxf(__uint_as_float(a), __uint_as_float(b)); }
#define XRED(name, OP) DEV float name(float x) { \
  auto s = __builtin_amdgcn_permlane16_swap(__float_as_uint(x), __float_as_uint(x), false, false); \
  x = OP(__uint_as_float(s[0]), __uint_as_float(s[1])); \
  auto t = __builtin_amdgcn_permlane32_swap(__float_as_uint(x), __float_as_uint(x), false, false); \
  return OP(__uint_as_float(t[0]), __uint_as_float(t[1])); }
DEV float addf(float a, float b) { return a + b; }
XRED(xsum, addf)
template <int N> DEV float ror(float x) { return __int_as_float(__builtin_amdgcn_update_dpp(0, __float_as_int(x), 0x120 + N, 0xf, 0xf, false)); }

DEV void glds(const void* g, unsigned lds) {
  unsigned keep;
  asm volatile("s_mov_b32 %0, m0\n\ts_mov_b32 m0, %2\n\ts_nop 0\n\tglobal_load_lds_dwordx4 %1, off\n\ts_mov_b32 m0, %0"
               : "=&s"(keep) : "v"(g), "s"(lds) : "memory");
}
DEV void touch(unsigned& d, const void* g) { asm volatile("global_load_dword %0, %1, off sc1" : "+v"(d) : "v"(g) : "memory"); }
DEV void keep(unsigned& d) { asm volatile("" : "+v"(d)); }
DEV void wait_vm0() { asm volatile("s_waitcnt vmcnt(0)" ::: "memory"); }
DEV void bar() { asm volatile("s_waitcnt lgkmcnt(0)\n\ts_barrier" ::: "memory"); }
typedef LAS char* lp;
DEV lp opq(unsigned a) { asm volatile("" : "+v"(a)); return (lp)a; }
DEV u32x4 fragp(lp a, int o, int d) {
  u32x2 x = __builtin_bit_cast(u32x2, __builtin_amdgcn_ds_read_tr16_b64_v4i16((LAS s16x4*)(a + o)));
  u32x2 y = __builtin_bit_cast(u32x2, __builtin_amdgcn_ds_read_tr16_b64_v4i16((LAS s16x4*)(a + o + d)));
  return u32x4{x[0], x[1], y[0], y[1]}; }
DEV f32x4 ldp4(lp a, int o) { return *(LAS f32x4*)(a + o); }
template <int I, int N, class F> DEV void sfor(F f) { if constexpr (I < N) { f(std::integral_constant<int, I>{}); sfor<I + 1, N>(f); } }
#define SB0() __builtin_amdgcn_sched_barrier(0)
template <int NM, int ND, int NV> DEV void ilv() {
#pragma unroll
  for (int i = 0; i < NM; ++i) { __builtin_amdgcn_sched_group_barrier(0x008, 1, 0); __builtin_amdgcn_sched_group_barrier(0x100, ND, 0);
    if (NV) __builtin_amdgcn_sched_group_barrier(0x002, NV, 0); }
}
DEV f32x4 ldsf4(unsigned a) { return *(LAS f32x4*)a; }
DEV void stsf4(unsigned a, f32x4 v) { *(LAS f32x4*)a = v; }

DEV void wait_flags(char* ws, unsigned first, unsigned stride, unsigned n, unsigned tag, int wave, int lane) {
  if (wave == 0) {
    gu32* p = (gu32*)(ws + WS_FLAG) + (first + (lane & (n - 1)) * stride) * 32;
#pragma unroll 1
    for (unsigned s = 0; s < SPIN_CAP; ++s) {
      if (__all(__hip_atomic_load(p, RLX_AGENT) == tag)) break;
      __builtin_amdgcn_s_sleep(1);
    }
  }
  bar();
}
DEV void publish(char* ws, unsigned idx, unsigned tag) {
  wait_vm0(); bar();
  if (threadIdx.x < 32) __hip_atomic_store((gu32*)(ws + WS_FLAG) + idx * 32 + threadIdx.x, tag, RLX_AGENT);
}
DEV u32x4 ldx(__amdgpu_buffer_rsrc_t rs, unsigned v, unsigned s) { return __builtin_amdgcn_raw_buffer_load_b128(rs, v, s, 16); }
#define ST(i)
#define ST2(i)

DEV void hedgehog(u32x4 wp, const float* v, u32x4& o0, u32x4& o1) {
  const float L = 1.4426950408889634f; f32x16 Z = {0}; f32x16 pp = mfma(wp, pk8(v), Z); float e[16];
#pragma unroll
  for (int s = 0; s < 2; ++s) {
    float m = fmaxf(fmaxf(fmaxf(pp[8 * s], pp[8 * s + 1]), fmaxf(pp[8 * s + 2], pp[8 * s + 3])), fmaxf(fmaxf(pp[8 * s + 4], pp[8 * s + 5]), fmaxf(pp[8 * s + 6], pp[8 * s + 7])));
    m = -hmax(m) * L; float t = 0;
#pragma unroll
    for (int i = 0; i < 8; ++i) { e[8 * s + i] = __builtin_amdgcn_exp2f(pp[8 * s + i] * L + m); t += e[8 * s + i]; }
    t = __builtin_amdgcn_rcpf(hsum(t));
#pragma unroll
    for (int i = 0; i < 8; ++i) e[8 * s + i] *= t;
  }
  o0 = pk8(e); o1 = pk8(e + 8);
}
DEV void reduce4(unsigned base, const f32x16 (&acc)[3], int wave, int lane, float* r) {
  const int wm = wave & 1, wq = wave >> 1;
#pragma unroll
  for (int d = 0; d < 4; ++d) {
    const f32x16 v = acc[d >> 1]; const int o = (d & 1) * 8;
    if (d != wq) { const unsigned a = base + ((d * 2 + wm) * 3 + ((wq - d - 1) & 3)) * 2048 + lane * 16;
      stsf4(a, f32x4{v[o], v[o + 1], v[o + 2], v[o + 3]}); stsf4(a + 1024, f32x4{v[o + 4], v[o + 5], v[o + 6], v[o + 7]}); }
    else {
#pragma unroll
      for (int i = 0; i < 8; ++i) r[i] = v[o + i]; }
  }
  bar();
#pragma unroll
  for (int s = 0; s < 6; ++s) { const f32x4 v = ldsf4(base + wave * 6144 + s * 1024 + lane * 16);
#pragma unroll
    for (int i = 0; i < 4; ++i) r[(s & 1) * 4 + i] += v[i]; }
}

__global__ void __launch_bounds__(512, 2)
adaln_block(const unsigned short* __restrict__ x, const unsigned short* __restrict__ wqkv, const unsigned short* __restrict__ wphi,
            const unsigned short* __restrict__ wo, const unsigned short* __restrict__ wg, const unsigned short* __restrict__ wu,
            const unsigned short* __restrict__ wd, const unsigned short* __restrict__ cin, const unsigned short* __restrict__ wmod,
            unsigned short* __restrict__ out, char* ws) {
  __shared__ __attribute__((aligned(16))) char smem[163840];
  const unsigned L0 = (unsigned)(unsigned long long)(LAS char*)smem;
  const int tid = threadIdx.x, lane = tid & 63, wave = __builtin_amdgcn_readfirstlane(tid >> 6);
  const int h = lane >> 5, r32 = lane & 31, cb = (lane >> 4) & 1, q = (lane & 15) >> 2, p = lane & 3;
  const int b = blockIdx.x, xc = b & 7, j = b >> 3;
  const int mt = (xc >> 1) * 4 + (j & 3), nt = (xc & 1) * 8 + (j >> 2);
  const int Mt = xc * 4 + (j & 3), Nt = j >> 2;
  const int m3 = (xc >> 1) * 2 + (j & 1), nf = (xc & 1) * 16 + (j >> 1);
  const int wm = wave & 3, wk = wave >> 2, wm2 = wave & 1, wq = wave >> 1;
  const __amdgpu_buffer_rsrc_t rs = __builtin_amdgcn_make_buffer_rsrc(ws, 0, WS_END, 0x00020000);
  const unsigned wbase = L0 + wave * 1024;
  const f32x16 Z16 = {0};
  ST(0);
  asm volatile("" :: "s"(wmod), "s"(cin), "s"(x), "s"(ws));
  unsigned tagv, cv[4]; u32x4 wv[4]; const int gb = xc * 12 + j;
  asm volatile("global_load_dword %0, %1, off sc1" : "=v"(tagv) : "v"(ws) : "memory");
  if (j < 12) {
#pragma unroll
    for (int i = 0; i < 4; ++i) { const int row = (tid >> 2) + 128 * i;
      asm volatile("global_load_dwordx4 %0, %2, off\n\tglobal_load_ushort %1, %3, off" : "=&v"(wv[i]), "=&v"(cv[i]) : "v"(wmod + row * 3072 + gb * 32 + p * 8), "v"(cin + row) : "memory"); } }
  unsigned pf = 0;
  touch(pf, x + (xc >> 1) * 262144 + (j * 128 + wave * 16 + (lane & 15)) * 64);
#pragma unroll
  for (int n = 0; n < 16; ++n) glds(x + (mt * 128 + wm * 32 + 2 * n + h) * 512 + wk * 256 + ((r32 ^ ((2 * n + h) & 15)) * 8), L0 + wave * 16384 + n * 1024);
  { const int l = r32 < 24 ? r32 : 0, r = l / 12, c = l - r * 12; touch(pf, wqkv + (j * 16 + wave * 2 + r) * 1536 + (c >> 2) * 512 + ((xc & 1) * 4 + (c & 3)) * 64); }
  asm volatile("s_waitcnt vmcnt(17)" : "+v"(tagv), "+v"(wv[0]), "+v"(wv[1]), "+v"(wv[2]), "+v"(wv[3]), "+v"(cv[0]), "+v"(cv[1]), "+v"(cv[2]), "+v"(cv[3]) :: "memory");
  u32x4 xa[32];
  const int u0 = nf < 22 ? 3 * nf : 2 * nf + 22, nu = nf < 22 ? 3 : 2, ul = u0 > 83 ? 83 : u0;
  const unsigned short *gsrc[3], *qsrc[3];
#pragma unroll
  for (int i = 0; i < 3; ++i) {
    const int c0 = i * 512 + tid, row = c0 / 12, cc = c0 - row * 12;
    qsrc[i] = wqkv + row * 1536 + (cc >> 2) * 512 + nt * 32 + (cc & 3) * 8;
  }
  auto load_gu = [&](int n0, int n1) {
#pragma unroll
    for (int n = n0; n < n1; ++n) glds(gsrc[n % 3] + (n / 3) * 128 * 1376, wbase + 49152 + n * 8192); };
  const int off8 = (wm2 * 8 + (lane >> 3)) * 512 + Nt * 64 + ((((lane >> 2) ^ (lane >> 4)) & 1) * 32) + p * 8;
  const int ks4 = wq * 22 - (wq == 3);
  auto load_wd = [&](int i0, int i1) {
#pragma unroll
    for (int i = i0; i < i1; ++i) glds(wd + off8 + (i == 21 && wq == 3 ? 85 : ks4 + i) * 8192, wbase + (i < 18 ? i : i - 18) * 8192); };

  auto load_wo = [&](int i0, int i1) {
#pragma unroll
    for (int i = i0; i < i1; ++i) glds(wo + off8 + (wq * 8 + i) * 8192, wbase + (i < 6 ? 98304 : 0) + i * 8192); };
  const unsigned tag = __builtin_amdgcn_readfirstlane(tagv) + 1u;
  if (j < 12) {
    f32x2 a[4] = {{0, 0}, {0, 0}, {0, 0}, {0, 0}};
#pragma unroll
    for (int i = 0; i < 4; ++i) {
      float c = lo16(cv[i]); c = rbf(c * __builtin_amdgcn_rcpf(1.f + __expf(-c)));
#pragma unroll
      for (int e = 0; e < 4; ++e) a[e] += c * f32x2{lo16(wv[i][e]), hi16(wv[i][e])};
    }
#pragma unroll
    for (int e = 0; e < 8; ++e) { float v = a[e >> 1][e & 1]; v += ror<4>(v); v += ror<8>(v); a[e >> 1][e & 1] = v; }
    if (q == 0) { const unsigned d = L0 + L_MOD + (tid >> 4) * 128 + p * 32;
      stsf4(d, f32x4{a[0][0], a[0][1], a[1][0], a[1][1]}); stsf4(d + 16, f32x4{a[2][0], a[2][1], a[3][0], a[3][1]}); }
    bar();
    if (wave == 0) { f32x2 s = {0, 0};
#pragma unroll
      for (int w = 0; w < 8; ++w) s += *(LAS f32x2*)(L0 + L_MOD + w * 512 + lane * 8);
      s[0] = xsum(s[0]); s[1] = xsum(s[1]);
      if (lane < 16) __hip_atomic_store((gu64*)(ws + WS_MOD) + gb * 16 + lane, ((unsigned long long)tag << 32) | pk2(s[0], s[1]), RLX_AGENT); } }
  asm volatile("s_waitcnt vmcnt(1)" ::: "memory");
  { const unsigned xb = L0 + wave * 16384 + r32 * 512 + ((h ^ (r32 & 15)) * 16);
#pragma unroll
    for (int st = 0; st < 16; ++st) xa[st] = *(LAS u32x4*)(xb ^ (st * 32)); }
  bar();
  ST(15);
  f32x2 t[64]; u32x4 g[4];
  auto ldg = [&]() {
#pragma unroll
    for (int i = 0; i < 4; ++i) g[i] = ldx(rs, WS_MOD + (wk * 256 + (lane & 15) * 16 + h * 8) * 4 + (i >> 1) * 2048 + (i & 1) * 16, 0); };
  { f32x2 sv = {0, 0}, vv = {0, 0};
#pragma unroll
    for (int s = 0; s < 4; ++s) {
#pragma unroll
      for (int i = 0; i < 3; ++i) glds(qsrc[i] + s * 128 * 1536, wbase + (s * 3 + i) * 8192);
      SB0();
#pragma unroll
      for (int i = 0; i < 16; ++i) { t[16 * s + i] = f32x2{lo16(xa[4 * s + (i >> 2)][i & 3]), hi16(xa[4 * s + (i >> 2)][i & 3])}; sv += t[16 * s + i]; vv += t[16 * s + i] * t[16 * s + i]; }
      SB0(); }
    ST(16);
    const float sh = hsum(sv[0] + sv[1]), vh = hsum(vv[0] + vv[1]);
    if (h == 0) *(LAS f32x2*)(L0 + L_SCR + 1024 + wave * 256 + r32 * 8) = f32x2{sh, vh};
    bar();
    ldg(); SB0();
    const f32x2 o = *(LAS f32x2*)(L0 + L_SCR + 1024 + (wave ^ 4) * 256 + r32 * 8);
    const float mean = (sh + o[0]) * (1.f / 512.f), rstd = rsqrtf(fmaxf((vh + o[1]) * (1.f / 512.f) - mean * mean, 0.f) + 1e-5f), dl = -mean * rstd;
#pragma unroll
    for (int i = 0; i < 64; ++i) { t[i] = t[i] * rstd + dl; asm volatile("" : "+v"(t[i])); } }
  ST(1);
  u32x4 wp;
  { const unsigned short* pp = wphi + (nt * 2 + wk) * 256 + 4 * h * 16 + (lane & 15); const unsigned sg = cb ? 0x80008000u : 0u;
#pragma unroll
    for (int e = 0; e < 4; ++e) { const int d = (e >> 1) * 8 + (e & 1) * 2; wp[e] = ((unsigned)pp[d * 16] | ((unsigned)pp[d * 16 + 16] << 16)) ^ sg; } }
  float ms[8], mc[8];
  {
#pragma unroll 1
    for (unsigned s = 0; s < SPIN_CAP; ++s) { bool ok = true;
#pragma unroll
      for (int i = 0; i < 4; ++i) ok = ok && g[i][1] == tag && g[i][3] == tag;
      if (__all(ok)) break;
      asm volatile("s_sleep 1" ::: "memory");
      ldg();
    }
#pragma unroll
    for (int i = 0; i < 4; ++i) { const unsigned u = g[i >> 1][(i & 1) * 2], v = g[2 + (i >> 1)][(i & 1) * 2], c = pk2(1.f + lo16(v), 1.f + hi16(v));
      ms[2 * i] = lo16(u); ms[2 * i + 1] = hi16(u); mc[2 * i] = lo16(c); mc[2 * i + 1] = hi16(c); } }
  asm volatile("s_waitcnt vmcnt(0)" : "+v"(wp), "+v"(pf) :: "memory"); bar();
  ST(2);
  { const int l = (wave - 2) * 64 + lane, sg = l / 12, o = (j * 32 + sg) >> 1;
    const int e = o * 1376 + (xc & 1) * 768 + (l - sg * 12) * 64;
    touch(pf, wave < 2 ? wo + (j * 128 + wave * 64 + lane) * 64 : ((sg & 1) ? wu : wg) + (e < 704510 ? e : 704510)); }
  f32x16 acc[3] = {Z16, Z16, Z16};
  { const lp wb = opq(L0 + (wk * 256 + h * 8 + q) * 192 + cb * 32 + p * 8);
    u32x4 w[2][3], hh[2];
    auto ldw = [&](int st, int c) { w[st & 1][c] = fragp(wb, st * 3072 + c * 64, 768); };
    auto mkh = [&](auto S, int e) {
      constexpr int st = S; float v0 = t[st * 4 + e][0], v1 = t[st * 4 + e][1];
      asm("v_mul_f32_dpp %0, %2, %0 row_newbcast:%6 row_mask:0xf bank_mask:0xf\n\tv_mul_f32_dpp %1, %3, %1 row_newbcast:%6 row_mask:0xf bank_mask:0xf\n\t"
          "v_add_f32_dpp %0, %4, %0 row_newbcast:%6 row_mask:0xf bank_mask:0xf\n\tv_add_f32_dpp %1, %5, %1 row_newbcast:%6 row_mask:0xf bank_mask:0xf"
          : "+v"(v0), "+v"(v1) : "v"(mc[2 * e]), "v"(mc[2 * e + 1]), "v"(ms[2 * e]), "v"(ms[2 * e + 1]), "i"(st));
      hh[st & 1][e] = pk2(v0, v1); };
#pragma unroll
    for (int e = 0; e < 8; ++e) asm volatile("" : "+v"(ms[e]), "+v"(mc[e]));
    asm volatile("s_nop 1"); SB0();
    ldw(0, 0); ldw(0, 1); ldw(0, 2);
#pragma unroll
    for (int e = 0; e < 4; ++e) mkh(std::integral_constant<int, 0>{}, e);
    SB0();
    sfor<0, 16>([&](auto S) { constexpr int st = S; std::integral_constant<int, (st + 1) & 15> N;
      acc[0] = mfma(w[st & 1][0], hh[st & 1], acc[0]); SB0();
      if (st < 15) { ldw(st + 1, 0); mkh(N, 0); mkh(N, 1); }
      SB0(); acc[1] = mfma(w[st & 1][1], hh[st & 1], acc[1]); SB0();
      if (st < 15) { ldw(st + 1, 1); mkh(N, 2); }
      SB0(); acc[2] = mfma(w[st & 1][2], hh[st & 1], acc[2]); SB0();
      if (st < 15) { ldw(st + 1, 2); mkh(N, 3); }
      if (st >= 5 && st < 11) load_wo(st - 5, st - 4);
      SB0(); }); }
  ST(3);
  u32x4 phq0, phq1, gm;
  { float v[3][8];
    bar(); load_wo(6, 8);
    auto send = [&](int o) {
#pragma unroll
      for (int s = 0; s < 6; ++s) { const f32x16 a = acc[s >> 1]; const int i = o + (s & 1) * 4; stsf4(L0 + wave * 6144 + s * 1024 + lane * 16, f32x4{a[i], a[i + 1], a[i + 2], a[i + 3]}); } };
    auto recv = [&](int o) {
#pragma unroll
      for (int s = 0; s < 6; ++s) { const f32x4 r = ldsf4(L0 + (wave ^ 4) * 6144 + s * 1024 + lane * 16);
#pragma unroll
        for (int i = 0; i < 4; ++i) v[s >> 1][(s & 1) * 4 + i] = acc[s >> 1][o + (s & 1) * 4 + i] + r[i]; } };
    if (wk) { send(0); bar(); recv(8); } else { send(8); bar(); recv(0); }
    ST(17);
    u32x4 k0, k1; hedgehog(wp, v[1], k0, k1);
    const u32x4 vb = pk8(v[2]);
    const unsigned img = L0 + 65536 + wave * 4096, wr = img + r32 * 104 + h * 8;
    *(LAS u32x2*)(wr) = u32x2{k0[0], k0[1]}; *(LAS u32x2*)(wr + 16) = u32x2{k0[2], k0[3]}; *(LAS u32x2*)(wr + 32) = u32x2{k1[0], k1[1]}; *(LAS u32x2*)(wr + 48) = u32x2{k1[2], k1[3]};
    *(LAS u32x2*)(wr + 64) = u32x2{vb[0], vb[1]}; *(LAS u32x2*)(wr + 80) = u32x2{vb[2], vb[3]};
    asm volatile("" ::: "memory");
    f32x16 kv = Z16; const lp ra = opq(img + (8 * h + q) * 104 + cb * 32 + p * 8), rb = opq(img + (8 * h + q) * 104 + 64 + p * 8);
#pragma unroll
    for (int s = 0; s < 2; ++s) { u32x4 bb = fragp(rb, s * 1664, 416);
#pragma unroll
      for (int e = 0; e < 4; ++e) bb[e] = cb ? 0x3f803f80u : bb[e];
      kv = mfma(fragp(ra, s * 1664, 416), bb, kv); }
    asm volatile("" ::: "memory");
    ST(18);
    if (r32 <= 16) {
#pragma unroll
      for (int c = 0; c < 4; ++c) stsf4(img + (c * 34 + h * 17 + r32) * 16, f32x4{kv[4 * c], kv[4 * c + 1], kv[4 * c + 2], kv[4 * c + 3]}); }
    bar();
    if (tid < 136) { const int c = tid / 34; const unsigned a = L0 + 65536 + (c >> 1) * 16384 + ((c & 1) * 68 + tid - c * 34) * 16;
      const f32x4 s = (ldsf4(a) + ldsf4(a + 4096)) + (ldsf4(a + 8192) + ldsf4(a + 12288)), r = (ldsf4(a + 544) + ldsf4(a + 4640)) + (ldsf4(a + 8736) + ldsf4(a + 12832));
      __builtin_amdgcn_raw_buffer_store_b128(u32x4{pk2(s[0], s[1]), pk2(s[2], s[3]), pk2(r[0], r[1]), pk2(r[2], r[3])}, rs, WS_KV + (nt * 16 + mt) * 2176 + tid * 16, 0, 16); }
    ST(20);
    publish(ws, mt * 16 + nt, tag); keep(pf);
    ST(4);
    gm = ldx(rs, WS_MOD + 4096 + tid * 16, 0);
    int tid2 = tid; asm volatile("" : "+v"(tid2));
#pragma unroll
    for (int i = 0; i < 3; ++i) { const int c0 = i * 512 + tid2, row = c0 / 12, cc = c0 - row * 12;
      gsrc[i] = ((cc & 2) ? wu : wg) + row * 1376 + (ul + (cc >> 2)) * 16 + (cc & 1) * 8; }
    hedgehog(wp, v[0], phq0, phq1); }
  const int tok2 = Mt * 64 + wm2 * 32 + r32, col2 = Nt * 64 + wq * 16;
  const u32x4 xr = *(const u32x4*)(x + tok2 * 512 + col2 + 8 * h);
  const unsigned wb8 = L0 + wq * 2048 + (4 * h + q) * 128 + cb * 32 + p * 8 + (q >> 1) * 64;
  u32x4 wf[8][2];
  { const lp wa[4] = {opq(wb8 + 98304), opq((wb8 ^ 64) + 98304), opq(wb8), opq(wb8 ^ 64)};
#pragma unroll
    for (int i = 0; i < 16; ++i) wf[i >> 1][i & 1] = fragp(wa[(i & 1) + (i < 12 ? 0 : 2)], (i >> 1) * 8192, 1024); }
#pragma unroll 1
  for (unsigned s = 0; s < SPIN_CAP; ++s) {
    if (__all(gm[1] == tag && gm[3] == tag)) break;
    asm volatile("s_sleep 1" ::: "memory");
    gm = ldx(rs, WS_MOD + 4096 + tid * 16, 0);
  }
  if (wq == 2) { gm[0] = pk2(1.f + lo16(gm[0]), 1.f + hi16(gm[0])); gm[2] = pk2(1.f + lo16(gm[2]), 1.f + hi16(gm[2])); }
  stsf4(L0 + L_MOD + 4096 + tid * 16, f32x4{lo16(gm[0]), hi16(gm[0]), lo16(gm[2]), hi16(gm[2])});
  if (wq == 1 || wq == 2) *(LAS u32x2*)(L0 + L_MOD + (2 - wq) * 1024 + (tid & 124) * 8 + (tid & 1) * 16 + (tid & 2) * 4) = u32x2{gm[0], gm[2]};
  load_gu(2, 6);
  wait_flags(ws, nt, 16, 16, tag, wave, lane);
  ST(5);
  { if (tid < 272) { u32x4 v[8]; const int hf = tid >= 136;
#pragma unroll
      for (int i = 0; i < 8; ++i) v[i] = ldx(rs, WS_KV + (nt * 16 + hf * 7) * 2176 + tid * 16, i * 2176);
      f32x2 s[4] = {{0, 0}, {0, 0}, {0, 0}, {0, 0}};
#pragma unroll
      for (int i = 0; i < 32; ++i) s[i & 3] += f32x2{lo16(v[i >> 2][i & 3]), hi16(v[i >> 2][i & 3])};
      stsf4(L0 + tid * 16, f32x4{s[0][0], s[0][1], s[1][0], s[1][1]}); stsf4(L0 + 4352 + tid * 16, f32x4{s[2][0], s[2][1], s[3][0], s[3][1]}); }
    bar();
    ST(22);
    const unsigned a = L0 + (wk * 68 + h * 17 + (r32 < 16 ? r32 : 16)) * 16; u32x4 ka[2];
#pragma unroll
    for (int i = 0; i < 4; ++i) { const unsigned o = a + (i >> 1) * 544 + (i & 1) * 4352; const f32x4 k = ldsf4(o) + ldsf4(o + 2176);
      ka[i >> 1][(i & 1) * 2] = pk2(k[0], k[1]); ka[i >> 1][(i & 1) * 2 + 1] = pk2(k[2], k[3]); }
    const f32x16 d = mfma(ka[1], phq1, mfma(ka[0], phq0, Z16));
    const float rd = __builtin_amdgcn_rcpf(d[8] + 1e-6f); float o[8];
#pragma unroll
    for (int i = 0; i < 8; ++i) o[i] = d[i] * rd;
    __builtin_amdgcn_raw_buffer_store_b128(pk8(o), rs, WS_O + ((mt * 4 + wm) * 32 + nt * 2 + wk) * 1024 + lane * 16, 0, 16);
    ST(24);
    publish(ws, 256 + mt * 16 + nt, tag); }
  ST(6);
  load_gu(0, 2); load_gu(6, 9);
  if (wave >= 2 && tid < 472) touch(pf, wd + (j * 344 + tid - 128) * 64);
  wait_flags(ws, 256 + (Mt >> 1) * 16, 1, 16, tag, wave, lane);
  if (b == 0 && tid == 0) __hip_atomic_store((gu32*)ws, tag, RLX_AGENT);
  ST(7);
  if (wave == 0) touch(pf, ws + WS_O + Mt * 65536 + (Nt * 64 + lane) * 128);
  u32x4 x1p;
  {
#pragma unroll
    for (int i = 0; i < 8; ++i) { xa[i] = ldx(rs, lane * 16, WS_O + ((Mt * 2 + wm2) * 32 + wq * 8 + i) * 1024); SB0(); }
    acc[0] = Z16; acc[1] = Z16;
#pragma unroll
    for (int i = 0; i < 8; ++i) { acc[0] = mfma(wf[i][0], xa[i], acc[0]); acc[1] = mfma(wf[i][1], xa[i], acc[1]); SB0(); }
    load_gu(9, 12);
    ST(25);
    float r[8]; reduce4(L0, acc, wave, lane, r);
    unsigned xs[4] = {xr[0], xr[1], xr[2], xr[3]}; swap32(xs[0], xs[2]); swap32(xs[1], xs[3]);
    const unsigned ma = L0 + L_MOD + (col2 + 4 * h) * 4; float y[8], s = 0, v = 0;
#pragma unroll
    for (int e = 0; e < 2; ++e) { const f32x4 ga = ldsf4(ma + 4096 + e * 32), ca = ldsf4(ma + 8192 + e * 32);
      x1p[2 * e] = pk2(lo16(xs[2 * e]) + ga[0] * r[4 * e], hi16(xs[2 * e]) + ga[1] * r[4 * e + 1]);
      x1p[2 * e + 1] = pk2(lo16(xs[2 * e + 1]) + ga[2] * r[4 * e + 2], hi16(xs[2 * e + 1]) + ga[3] * r[4 * e + 3]);
#pragma unroll
      for (int i = 0; i < 4; ++i) { r[4 * e + i] = (i & 1) ? hi16(x1p[2 * e + (i >> 1)]) : lo16(x1p[2 * e + (i >> 1)]); y[4 * e + i] = r[4 * e + i] * ca[i]; s += r[4 * e + i]; } }
    __builtin_amdgcn_raw_buffer_store_b128(pk8(y), rs, WS_X1 + ((Mt * 2 + wm2) * 32 + Nt * 4 + wq) * 1024 + lane * 16, 0, 16);
    s = hsum(s) * (1.f / 16.f);
#pragma unroll
    for (int i = 0; i < 8; ++i) v += (r[i] - s) * (r[i] - s);
    v = hsum(v);
    if (h == 0) *(LAS f32x2*)(L0 + L_SCR + wave * 256 + r32 * 8) = f32x2{s, v};
    bar();
    if (tid < 64) { f32x2 a[4];
#pragma unroll
      for (int i = 0; i < 4; ++i) a[i] = *(LAS f32x2*)(L0 + L_SCR + i * 512 + tid * 8);
      const float m = ((a[0][0] + a[1][0]) + (a[2][0] + a[3][0])) * 0.25f; float M = 0;
#pragma unroll
      for (int i = 0; i < 4; ++i) M += a[i][1] + 16.f * (a[i][0] - m) * (a[i][0] - m);
      __builtin_amdgcn_raw_buffer_store_b64(u32x2{__float_as_uint(m), __float_as_uint(M)}, rs, WS_ST + (((Mt * 2 + h) * 8 + Nt) * 32 + r32) * 8, 0, 16); }
    ST(26);
    publish(ws, 512 + Mt * 8 + Nt, tag); keep(pf); }
  ST(8);
  load_wd(0, 6);
  const unsigned wb3 = L0 + 49152 + (4 * h + q) * 192 + cb * 32 + p * 8;
  if (wave >= 2) { f32x16 d = Z16; u32x4 fa[16], fb[16];
    const lp wpt = opq(wb3 + (wq - 1) * 64 + wm2 * 49152), mp = opq(L0 + L_MOD + (r32 == 1) * 1024 + wm2 * 512 + h * 16);
#pragma unroll
    for (int s = 0; s < 16; ++s) { fa[s] = *(LAS u32x4*)(mp + s * 32); fb[s] = fragp(wpt, s * 3072, 1536); }
    SB0();
#pragma unroll
    for (int s = 0; s < 16; ++s) d = mfma(fa[s], fb[s], d);
    if (h == 0) *(LAS f32x2*)(L0 + L_SCR + (wave - 2) * 256 + r32 * 8) = f32x2{d[0], d[1]}; }
  ST(28);
  wait_flags(ws, 512 + m3 * 32, 1, 32, tag, wave, lane);
  ST(9);
  if (wave < 2) touch(pf, ws + WS_X1 + m3 * 262144 + ((j >> 1) * 128 + tid) * 128);
  { const int tt = m3 * 8 + wave;
    auto lda = [&](int s) { xa[s] = ldx(rs, lane * 16, WS_X1 + (tt * 32 + s) * 1024); };
#pragma unroll
    for (int s = 0; s < D3; ++s) { lda(s); SB0(); }
    u32x2 su[4];
#pragma unroll
    for (int i = 0; i < 4; ++i) su[i] = __builtin_amdgcn_raw_buffer_load_b64(rs, ((4 * h + i) * 32 + r32) * 8, WS_ST + tt * 2048, 16);
    unsigned uv[3][3];
#pragma unroll
    for (int T = 0; T < 3; ++T) { const f32x2 a = *(LAS f32x2*)(L0 + L_SCR + T * 512 + r32 * 8) + *(LAS f32x2*)(L0 + L_SCR + T * 512 + 256 + r32 * 8);
      const float uh = rbf(a[0]), vh = rbf(a[1]); uv[T][0] = pk2(uh, uh); uv[T][1] = pk2(a[0] - uh, vh); uv[T][2] = pk2(vh, a[1] - vh); }
    acc[0] = Z16; acc[1] = Z16; acc[2] = Z16;
    const lp wpp[2] = {opq(wb3), opq(wb3 + 49152)}; u32x4 w[2][3];
    auto ldw = [&](int s) {
#pragma unroll
      for (int c = 0; c < 3; ++c) w[s & 1][c] = fragp(wpp[s >> 4], (s & 15) * 3072 + c * 64, 1536); };
    ST2(21);
    ldw(0); SB0();
#pragma unroll
    for (int s = 0; s < 32; ++s) {
      if (s == 15) bar();
      if (s + D3 < 32) lda(s + D3);
      if (s == 26) { asm volatile("" : "+v"(xa[26]), "+v"(xa[27]), "+v"(xa[28]), "+v"(xa[29]), "+v"(xa[30]), "+v"(xa[31]), "+v"(su[0]), "+v"(su[1]), "+v"(su[2]), "+v"(su[3])); load_wd(6, 12); }
      if (s < 31) ldw(s + 1);
#pragma unroll
      for (int c = 0; c < 3; ++c) acc[c] = mfma(w[s & 1][c], xa[s], acc[c]);
      ilv<3, 2, 0>(); SB0();
    }
    ST(10);
    const f32x4 s0 = __builtin_bit_cast(f32x4, u32x4{su[0][0], su[0][1], su[1][0], su[1][1]}), s1 = __builtin_bit_cast(f32x4, u32x4{su[2][0], su[2][1], su[3][0], su[3][1]});
    const float mean = hsum((s0[0] + s0[2]) + (s1[0] + s1[2])) * 0.125f;
    const float d0 = s0[0] - mean, d1 = s0[2] - mean, d2 = s1[0] - mean, d3 = s1[2] - mean;
    const float var = hsum((s0[1] + s0[3]) + (s1[1] + s1[3]) + 64.f * (d0 * d0 + d1 * d1 + d2 * d2 + d3 * d3)) * (1.f / 512.f);
    const float rstd = rsqrtf(var + 1e-5f), sd = (var + 1e-5f) * rstd, ah = rbf(-mean), dh = rbf(sd), cg = -1.4426950408889634f * rstd;
    const u32x4 bx = {h ? 0u : pk2(ah, -mean - ah), h ? 0u : pk2(ah, dh), h ? 0u : pk2(sd - dh, dh), 0u};
#pragma unroll
    for (int T = 0; T < 3; ++T) acc[T] = mfma(u32x4{uv[T][0], uv[T][1], uv[T][2], 0u}, bx, acc[T]);
#pragma unroll
    for (int T = 0; T < 3; ++T) { float r[8];
#pragma unroll
      for (int i = 0; i < 8; ++i) r[i] = (acc[T][8 + i] * rstd) * __builtin_amdgcn_rcpf(1.f + __builtin_amdgcn_exp2f(cg * acc[T][i]));
      if (ul + T >= u0 && ul + T < u0 + nu) __builtin_amdgcn_raw_buffer_store_b128(pk8(r), rs, WS_H + (tt * 86 + ul + T) * 1024 + lane * 16, 0, 16);
    }
    ST(29);
    publish(ws, 768 + m3 * 32 + nf, tag); keep(pf); }
  ST(11);
  load_wd(12, 18);
  wait_flags(ws, 768 + (Mt >> 2) * 32, 1, 32, tag, wave, lane);
  ST(12);
  if (tid < 172) touch(pf, ws + WS_H + Mt * 176128 + (Nt * 172 + tid) * 128);
  { const unsigned hb = WS_H + ((Mt * 2 + wm2) * 86 + ks4) * 1024;
    auto lda = [&](int i) { xa[i] = ldx(rs, lane * 16, hb + (i == 21 && wq >= 2 ? 20 : i) * 1024); };
#pragma unroll
    for (int i = 0; i < D4; ++i) { lda(i); SB0(); }
    acc[0] = Z16; acc[1] = Z16;
    lp wpp[3][2]; u32x4 w[2][2];
#pragma unroll
    for (int i = 0; i < 6; ++i) wpp[i >> 1][i & 1] = opq((wb8 ^ ((i & 1) * 64)) + (i >> 1) * 49152);
    auto ldw = [&](int i) { const int sl = i < 18 ? i : i - 18; w[i & 1][0] = fragp(wpp[sl / 6][0], (sl % 6) * 8192, 1024); w[i & 1][1] = fragp(wpp[sl / 6][1], (sl % 6) * 8192, 1024); };
    ST2(23);
    ldw(0); SB0();
#pragma unroll
    for (int i = 0; i < 22; ++i) {
      if (i == 3) { bar(); load_wd(18, 22); }
      if (i + D4 < 22) lda(i + D4);
      if (i == 17) { wait_vm0(); keep(pf); bar(); }
      if (i < 21) ldw(i + 1);
      if (i == 21 && wq >= 2) xa[21] = u32x4{0, 0, 0, 0};
      acc[0] = mfma(w[i & 1][0], xa[i], acc[0]); acc[1] = mfma(w[i & 1][1], xa[i], acc[1]);
      ilv<2, 2, 0>(); SB0();
    }
    ST(13);
    float r[8]; reduce4(L0 + 49152, acc, wave, lane, r);
    unsigned o[4];
#pragma unroll
    for (int e = 0; e < 2; ++e) { const f32x4 gv = ldsf4(L0 + L_MOD + (2560 + col2 + 4 * h) * 4 + e * 32);
      o[2 * e] = pk2(lo16(x1p[2 * e]) + gv[0] * r[4 * e], hi16(x1p[2 * e]) + gv[1] * r[4 * e + 1]);
      o[2 * e + 1] = pk2(lo16(x1p[2 * e + 1]) + gv[2] * r[4 * e + 2], hi16(x1p[2 * e + 1]) + gv[3] * r[4 * e + 3]); }
    swap32(o[0], o[2]); swap32(o[1], o[3]);
    *(LAS u32x4*)(L0 + 131072 + (wm2 * 32 + r32) * 144 + wq * 32 + h * 16) = u32x4{o[0], o[1], o[2], o[3]};
    bar();
    __builtin_amdgcn_raw_buffer_store_b128(*(LAS u32x4*)(L0 + 131072 + (tid >> 3) * 144 + (tid & 7) * 16), __builtin_amdgcn_make_buffer_rsrc(out, 0, 2097152, 0x00020000),
                                           (Mt * 64 + (tid >> 3)) * 1024 + Nt * 128 + (tid & 7) * 16, 0, 19);
  }
  ST(14);
}

extern "C" void kernel_launch(void* const* d_in, const int* in_sizes, int n_in, void* d_out, int out_size, void* d_ws, size_t ws_size,
                              hipStream_t stream) {
  typedef const unsigned short* P;
  adaln_block<<<256, 512, 0, stream>>>((P)d_in[0], (P)d_in[1], (P)d_in[2], (P)d_in[3], (P)d_in[5], (P)d_in[6], (P)d_in[7], (P)d_in[8], (P)d_in[9],
                                       (unsigned short*)d_out, (char*)d_ws);
}
```
